```python
import torch

def _softmax(x, axis=-1):
    return torch.softmax(x, dim=axis)

def _gelu(x):
    return torch.nn.functional.gelu(x, approximate='tanh')

def _silu(x):
    return torch.nn.functional.silu(x)

def _sigmoid(x):
    return torch.sigmoid(x)

def _layernorm(x, axis=-1, eps=1e-05):
    m = x.mean(dim=axis, keepdim=True)
    v = x.var(dim=axis, keepdim=True, unbiased=False)
    return (x - m) / torch.sqrt(v + eps)

def _scatter_add(tgt, idx, src, axis=0):
    return tgt.clone().index_add_(axis, idx, src)

def _segment_sum(src, seg, n):
    out = torch.zeros((n,) + src.shape[1:], dtype=src.dtype)
    return out.index_add_(0, seg, src)

def _pad(x, pw):
    flat = tuple((v for pair in reversed(pw) for v in pair))
    return torch.nn.functional.pad(x, flat)
kernel_name = 'adaLN_cond__ssm_conv_h3__dense_activation_squared_relu__prompt'

def setup_inputs():

    _w = lambda a: (lambda t: t.to(torch.bfloat16) if t.is_floating_point() else t)(torch.as_tensor(a))

    def _A_setup():
        B, S, D = (1, 2048, 512)
        ks = 4
        return {'q': _w((lambda *s: torch.randn(*s, dtype=torch.bfloat16) * 0.05)(B, S, D)), 'k': _w((lambda *s: torch.randn(*s, dtype=torch.bfloat16) * 0.8)(B, S, D)), 'v': _w((lambda *s: torch.randn(*s, dtype=torch.bfloat16) * 0.8)(B, S, D)), 'shift_k': _w((lambda *s: torch.randn(*s, dtype=torch.bfloat16) * 0.8)(D, ks) * 0.5), 'ssm_k': _w((lambda *s: torch.randn(*s, dtype=torch.bfloat16) * 0.8)(D, S) * (1.0 / S ** 0.5)), 'ssm_D': _w((lambda *s: torch.randn(*s, dtype=torch.bfloat16) * 0.8)(D))}
    _A_in = _A_setup()

    def _B_setup():
        B, S, D, F = (1, 2048, 512, 1376)
        return {'x': _w((lambda *s: torch.randn(*s, dtype=torch.bfloat16) * 0.05)(B, S, D)), 'w1': _w((lambda *s: torch.randn(*s, dtype=torch.bfloat16) * 0.05)(D, F)), 'b1': _w((lambda *s: torch.randn(*s, dtype=torch.bfloat16) * 0.4)(F)), 'w2': _w((lambda *s: torch.randn(*s, dtype=torch.bfloat16) * 0.05)(F, D)), 'b2': _w((lambda *s: torch.randn(*s, dtype=torch.bfloat16) * 3.2)(D))}
    _B_in = _B_setup()
    return {'A_q': _A_in['q'], 'A_k': _A_in['k'], 'A_v': _A_in['v'], 'A_shift_k': _A_in['shift_k'], 'A_ssm_k': _A_in['ssm_k'], 'A_ssm_D': _A_in['ssm_D'], 'B_x': _B_in['x'], 'B_w1': _B_in['w1'], 'B_b1': _B_in['b1'], 'B_w2': _B_in['w2'], 'B_b2': _B_in['b2'], 'c': _w((lambda *s: torch.randn(*s, dtype=torch.bfloat16) * 0.1)(1, 512) * 8.0), 'w_mod': _w((lambda *s: torch.randn(*s, dtype=torch.bfloat16) * 0.1)(512, 6 * 512) * 512 ** (-0.5) * 8.0)}

def reference(A_q, A_k, A_v, A_shift_k, A_ssm_k, A_ssm_D, B_x, B_w1, B_b1, B_w2, B_b2, c, w_mod):

    def _A_ref(q, k, v, shift_k, ssm_k, ssm_D):
        B, S, D = q.shape
        ks = shift_k.shape[-1]
        k_pad = _pad(k, ((0, 0), (ks - 1, 0), (0, 0)))
        k_shift = torch.zeros_like(k)
        for t in range(ks):
            k_shift = k_shift + k_pad[:, t:t + S, :] * shift_k[:, ks - 1 - t][None, None, :]
        kv = k_shift * v
        L = 2 * S
        kv_pad = _pad(kv, ((0, 0), (0, S), (0, 0)))
        kf_pad = _pad(ssm_k, ((0, 0), (0, S)))
        kv_f = (lambda x, axes=(-2, -1): torch.fft.rfft2(x.float(), dim=axes))(kv_pad[:, None, :, :], axes=(1, 2))
        kf_f = (lambda x, axes=(-2, -1): torch.fft.rfft2(x.float(), dim=axes))(kf_pad[:, None, :], axes=(1, 2))
        kf_f = (lambda x, *d: x.permute(*d))(kf_f, 1, 2, 0)
        y_f = kv_f * kf_f[None, :, :, :]
        y = (lambda x, s, axes=(-2, -1): torch.fft.irfft2(x, s=s, dim=axes).to(torch.bfloat16))(y_f, (1, L), axes=(1, 2))[:, 0, :S, :]
        y = y + kv * ssm_D[None, None, :]
        return q * y

    def _B_ref(x, w1, b1, w2, b2):
        h = torch.einsum('bsd,df->bsf', x, w1) + b1
        zero = torch.zeros_like(h)
        r = torch.where(h > zero, h, zero)
        h = r * r
        out = torch.einsum('bsf,fd->bsd', h, w2) + b2
        return out
    x = A_q
    mod = (_silu(c) @ w_mod).reshape(c.shape[0], 6, 1, -1)
    sa, ca, ga, sb, cb, gb = (mod[:, 0].clone(), mod[:, 1].clone(), mod[:, 2].clone(), mod[:, 3].clone(), mod[:, 4].clone(), mod[:, 5].clone())
    h = _layernorm(x, axis=-1) * (1 + ca) + sa
    x = x + ga * _A_ref(h, A_k, A_v, A_shift_k, A_ssm_k, A_ssm_D)
    h = _layernorm(x, axis=-1) * (1 + cb) + sb
    x = x + gb * _B_ref(h, B_w1, B_b1, B_w2, B_b2)
    return x
```

```cpp
#include <hip/hip_runtime.h>
#include <hip/hip_bf16.h>
#include <stdint.h>

typedef unsigned short u16;
typedef unsigned int u32;
typedef u32 v4u __attribute__((ext_vector_type(4)));
typedef u32 v2u __attribute__((ext_vector_type(2)));
typedef float v4f __attribute__((ext_vector_type(4)));
typedef float v2f __attribute__((ext_vector_type(2)));
typedef short v4s __attribute__((ext_vector_type(4)));
typedef __bf16 v8bf __attribute__((ext_vector_type(8)));
typedef __bf16 v2bf __attribute__((ext_vector_type(2)));
#define LAS __attribute__((address_space(3)))
#define DI __device__ __forceinline__

constexpr int S_ = 2048, D_ = 512, F_ = 1376;
constexpr int NFT = F_ / 16;
constexpr int NK2 = F_ / 32;

constexpr size_t OFF_KVT = 0;
constexpr size_t OFF_XT  = OFF_KVT + (size_t)D_ * S_ * 2;
constexpr size_t OFF_X1T = OFF_XT + (size_t)D_ * S_ * 2;
constexpr size_t OFF_ST1 = OFF_X1T + (size_t)D_ * S_ * 2;
constexpr size_t OFF_MOD = OFF_ST1 + (size_t)S_ * 8;
constexpr size_t OFF_TP  = OFF_MOD + (size_t)4 * 3072 * 4;
constexpr size_t OFF_W1T = OFF_TP + (size_t)F_ * 64;
constexpr size_t OFF_W2T = OFF_W1T + (size_t)D_ * F_ * 2;
constexpr size_t OFF_HID = OFF_W2T + (size_t)D_ * F_ * 2;
constexpr size_t WS_END  = OFF_HID + (size_t)S_ * F_ * 2;

DI float bf_lo(u32 w) { return __builtin_bit_cast(float, w << 16); }
DI float bf_hi(u32 w) { return __builtin_bit_cast(float, w & 0xffff0000u); }
DI float bf1(u16 h) { return __builtin_bit_cast(float, (u32)h << 16); }
DI u32 pack2(float a, float b) {
  v2f f = {a, b};
  return __builtin_bit_cast(u32, __builtin_convertvector(f, v2bf));
}
DI float rbf(float a) { return bf_lo(pack2(a, a)); }
DI v4f mfma16(v4u a, v4u b, v4f c) {
  return __builtin_amdgcn_mfma_f32_16x16x32_bf16(__builtin_bit_cast(v8bf, a), __builtin_bit_cast(v8bf, b), c, 0, 0, 0);
}
template <int CTRL> DI float dpp_add(float v) {
  int t = __builtin_amdgcn_update_dpp(0, __builtin_bit_cast(int, v), CTRL, 0xf, 0xf, true);
  return v + __builtin_bit_cast(float, t);
}
DI float xor16_add(float v) {
  u32 u = __builtin_bit_cast(u32, v);
  auto r = __builtin_amdgcn_permlane16_swap(u, u, false, false);
  return __builtin_bit_cast(float, (u32)r[0]) + __builtin_bit_cast(float, (u32)r[1]);
}
DI float xor32_add(float v) {
  u32 u = __builtin_bit_cast(u32, v);
  auto r = __builtin_amdgcn_permlane32_swap(u, u, false, false);
  return __builtin_bit_cast(float, (u32)r[0]) + __builtin_bit_cast(float, (u32)r[1]);
}
DI float wave_sum(float v) {
  v = dpp_add<0xB1>(v);
  v = dpp_add<0x4E>(v);
  v = dpp_add<0x141>(v);
  v = dpp_add<0x140>(v);
  v = xor16_add(v);
  v = xor32_add(v);
  return v;
}
DI float dot2(u32 a, u32 b, float c) {
  return __builtin_amdgcn_fdot2_f32_bf16(__builtin_bit_cast(v2bf, a), __builtin_bit_cast(v2bf, b), c, false);
}
DI v2f ln_stats(float mean, float var) {
  float m = rbf(mean);
  float v = rbf(var);
  float sd = rbf(__builtin_sqrtf(rbf(v + 1e-5f)));
  v2f r = {m, 1.0f / sd};
  return r;
}
DI float mod_val(const float* modp, int idx) {
  return rbf((modp[idx] + modp[3072 + idx]) + (modp[2 * 3072 + idx] + modp[3 * 3072 + idx]));
}

constexpr int K1_A = 256, K1_B = 192, K1_C = NK2 * 4;
__global__ __launch_bounds__(256) void k1_prep(const u16* __restrict__ x, const u16* __restrict__ k,
                                               const u16* __restrict__ v, const u16* __restrict__ shk,
                                               const u16* __restrict__ cvec, const u16* __restrict__ wmod,
                                               const u16* __restrict__ w2, char* __restrict__ ws) {
  __shared__ float red[4][64];
  const int b = blockIdx.x, t = threadIdx.x, l = t & 63, w = t >> 6;
  if (b < K1_A) {
    const int g = b & 7, j = b >> 3;
    const int cp = 8 * w + (l & 7), so = l >> 3;
    const int d0 = 64 * g + 2 * cp;
    const int s0 = 64 * j + 8 * so;
    u32 kk[11], vv[8], xx[8];
#pragma unroll
    for (int i = 0; i < 11; ++i) {
      int r = s0 - 3 + i;
      int rc = r < 0 ? 0 : r;
      u32 val = *(const u32*)(k + (size_t)rc * D_ + d0);
      kk[i] = r < 0 ? 0u : val;
    }
#pragma unroll
    for (int i = 0; i < 8; ++i) vv[i] = *(const u32*)(v + (size_t)(s0 + i) * D_ + d0);
#pragma unroll
    for (int i = 0; i < 8; ++i) xx[i] = *(const u32*)(x + (size_t)(s0 + i) * D_ + d0);
    const v4u sk = *(const v4u*)(shk + d0 * 4);
    const int r0 = 8 * b + 2 * w;
    const v4u xa = *(const v4u*)(x + (size_t)r0 * D_ + l * 8);
    const v4u xb = *(const v4u*)(x + (size_t)(r0 + 1) * D_ + l * 8);

    const float a0 = bf_lo(sk[0]), a1 = bf_hi(sk[0]), a2 = bf_lo(sk[1]), a3 = bf_hi(sk[1]);
    const float b0 = bf_lo(sk[2]), b1 = bf_hi(sk[2]), b2 = bf_lo(sk[3]), b3 = bf_hi(sk[3]);
    float oa[8], ob[8];
#pragma unroll
    for (int i = 0; i < 8; ++i) {
      float sa = bf_lo(kk[i]) * a3 + bf_lo(kk[i + 1]) * a2 + bf_lo(kk[i + 2]) * a1 + bf_lo(kk[i + 3]) * a0;
      float sb = bf_hi(kk[i]) * b3 + bf_hi(kk[i + 1]) * b2 + bf_hi(kk[i + 2]) * b1 + bf_hi(kk[i + 3]) * b0;
      u32 p = pack2(sa, sb);
      oa[i] = bf_lo(p) * bf_lo(vv[i]);
      ob[i] = bf_hi(p) * bf_hi(vv[i]);
    }
    v4u ra, rb, xa4, xb4;
#pragma unroll
    for (int i = 0; i < 4; ++i) {
      ra[i] = pack2(oa[2 * i], oa[2 * i + 1]);
      rb[i] = pack2(ob[2 * i], ob[2 * i + 1]);
      xa4[i] = (xx[2 * i] & 0xffffu) | (xx[2 * i + 1] << 16);
      xb4[i] = (xx[2 * i] >> 16) | (xx[2 * i + 1] & 0xffff0000u);
    }
    u16* kvT = (u16*)(ws + OFF_KVT);
    u16* xT = (u16*)(ws + OFF_XT);
    *(v4u*)(kvT + (size_t)d0 * S_ + s0) = ra;
    *(v4u*)(kvT + (size_t)(d0 + 1) * S_ + s0) = rb;
    *(v4u*)(xT + (size_t)d0 * S_ + s0) = xa4;
    *(v4u*)(xT + (size_t)(d0 + 1) * S_ + s0) = xb4;
    float s1 = 0.f, s2 = 0.f;
#pragma unroll
    for (int i = 0; i < 4; ++i) {
      s1 += bf_lo(xa[i]) + bf_hi(xa[i]);
      s2 += bf_lo(xb[i]) + bf_hi(xb[i]);
    }
    const float m1 = wave_sum(s1) * (1.0f / D_), m2 = wave_sum(s2) * (1.0f / D_);
    float q1 = 0.f, q2 = 0.f;
#pragma unroll
    for (int i = 0; i < 4; ++i) {
      float e;
      e = bf_lo(xa[i]) - m1; q1 += e * e;
      e = bf_hi(xa[i]) - m1; q1 += e * e;
      e = bf_lo(xb[i]) - m2; q2 += e * e;
      e = bf_hi(xb[i]) - m2; q2 += e * e;
    }
    q1 = wave_sum(q1) * (1.0f / D_);
    q2 = wave_sum(q2) * (1.0f / D_);
    if (l == 0) {
      v2f sA = ln_stats(m1, q1), sB = ln_stats(m2, q2);
      v4f o = {sA[0], sA[1], sB[0], sB[1]};
      *(v4f*)(ws + OFF_ST1 + (size_t)r0 * 8) = o;
    }
  } else if (b < K1_A + K1_B) {
    const int u = b - K1_A;
    const int cg = u >> 2, ks = u & 3;
    const int l8 = t & 7, rg = t >> 3;
    v4u wv[4];
    float sc[4];
#pragma unroll
    for (int i = 0; i < 4; ++i) {
      int r = ks * 128 + rg + 32 * i;
      wv[i] = *(const v4u*)(wmod + (size_t)r * 3072 + cg * 64 + l8 * 8);
      sc[i] = bf1(cvec[r]);
    }
    float acc[8];
#pragma unroll
    for (int e = 0; e < 8; ++e) acc[e] = 0.f;
#pragma unroll
    for (int i = 0; i < 4; ++i) {
      float c = sc[i];
      float s = rbf(c / (1.0f + __expf(-c)));
#pragma unroll
      for (int e = 0; e < 4; ++e) {
        acc[2 * e] += s * bf_lo(wv[i][e]);
        acc[2 * e + 1] += s * bf_hi(wv[i][e]);
      }
    }
#pragma unroll
    for (int e = 0; e < 8; ++e) {
      float a = dpp_add<0x128>(acc[e]);
      a = xor16_add(a);
      a = xor32_add(a);
      acc[e] = a;
    }
    if (l < 8) {
#pragma unroll
      for (int e = 0; e < 8; ++e) red[w][l * 8 + e] = acc[e];
    }
    __syncthreads();
    if (t < 64) {
      float* modp = (float*)(ws + OFF_MOD);
      modp[ks * 3072 + cg * 64 + t] = (red[0][t] + red[1][t]) + (red[2][t] + red[3][t]);
    }
  } else {
    const int u = b - K1_A - K1_B;
    const int ks = u >> 2, g = u & 3;
    const int d = 2 * t;
    u32 wv[8];
#pragma unroll
    for (int j = 0; j < 8; ++j) {
      int f = 32 * ks + 16 * (j >> 2) + 4 * g + (j & 3);
      wv[j] = *(const u32*)(w2 + (size_t)f * D_ + d);
    }
    v4u lo, hi;
#pragma unroll
    for (int i = 0; i < 4; ++i) {
      lo[i] = (wv[2 * i] & 0xffffu) | (wv[2 * i + 1] << 16);
      hi[i] = (wv[2 * i] >> 16) | (wv[2 * i + 1] & 0xffff0000u);
    }
    const int blk = d >> 6, dl = d & 63;
    const int m = 4 * (dl >> 4) + (dl & 3), dt = 4 * blk + ((dl >> 2) & 3);
    char* dst = ws + OFF_W2T + ((size_t)(ks * 32 + dt) * 64 + g * 16 + m) * 16;
    *(v4u*)dst = lo;
    *(v4u*)(dst + 16) = hi;
  }
}

constexpr int CP_STRIDE = 4160;
constexpr int CH_FILT = 8 * CP_STRIDE;
constexpr int U_BYTES = 512 + 4096;
constexpr int K2_LDS_U = 2 * CH_FILT;
constexpr int K2_LDS = K2_LDS_U + 2 * U_BYTES;
constexpr int K2_A = 256, K2_B = 22 * 8;

__global__ __launch_bounds__(256, 2) void k2_conv(const u16* __restrict__ ssmk, const u16* __restrict__ ssmD,
                                                  const u16* __restrict__ w1, const u16* __restrict__ b1,
                                                  char* __restrict__ ws) {
  __shared__ __attribute__((aligned(16))) char lds[K2_LDS];
  const int b = blockIdx.x, t = threadIdx.x, l = t & 63;
  const int w = __builtin_amdgcn_readfirstlane(t >> 6);
  const float* modp = (const float*)(ws + OFF_MOD);
  if (b < K2_A) {
    const int ch = w >> 1, wh = w & 1;
    const int d = 64 * (b & 7) + 2 * (b >> 3) + ch;
    const int tc = t & 127;
    const u16* kvT = (const u16*)(ws + OFF_KVT) + (size_t)d * S_;
    const u16* hf = ssmk + (size_t)d * S_;
    v4u u0 = *(const v4u*)(kvT + tc * 8), u1 = *(const v4u*)(kvT + 1024 + tc * 8);
    v4u hi0 = *(const v4u*)(hf + 2040 - 8 * tc), lo0 = *(const v4u*)(hf + 2032 - 8 * tc);
    v4u hi1 = *(const v4u*)(hf + 1016 - 8 * tc);
    v4u lo1 = *(const v4u*)(hf + (tc == 127 ? 0 : 1008 - 8 * tc));
    if (tc == 127) lo1 = v4u{0u, 0u, 0u, 0u};
    const int c = l & 15, g = l >> 4;
    const int sl = 1024 * wh + 16 * c + 4 * g;
    v2u xq[4];
    v4f st[4][2];
    const u16* xT = (const u16*)(ws + OFF_XT) + (size_t)d * S_;
#pragma unroll
    for (int n = 0; n < 4; ++n) {
      xq[n] = *(const v2u*)(xT + sl + 256 * n);
      st[n][0] = *(const v4f*)(ws + OFF_ST1 + (size_t)(sl + 256 * n) * 8);
      st[n][1] = *(const v4f*)(ws + OFF_ST1 + (size_t)(sl + 256 * n) * 8 + 16);
    }
    const float sa = mod_val(modp, d), ca1 = rbf(1.0f + mod_val(modp, 512 + d)), ga = mod_val(modp, 1024 + d);
    const float dd = bf1(ssmD[d]);
    char* fb = lds + ch * CH_FILT;
    char* ub = lds + K2_LDS_U + ch * U_BYTES;
    auto build = [&](v4u lo, v4u hi, int cidx) {
      u32 wd[8] = {lo[0], lo[1], lo[2], lo[3], hi[0], hi[1], hi[2], hi[3]};
      u32 R[16];
#pragma unroll
      for (int n = 2; n < 16; ++n) {
        if (n & 1) R[n] = __builtin_amdgcn_alignbit(wd[n >> 1], wd[n >> 1], 16);
        else R[n] = (wd[n >> 1] & 0xffffu) | (wd[(n >> 1) - 1] & 0xffff0000u);
      }
#pragma unroll
      for (int q = 0; q < 8; ++q) {
        v4u o = {R[8 + q], R[6 + q], R[4 + q], R[2 + q]};
        *(v4u*)(fb + q * CP_STRIDE + cidx * 16) = o;
      }
    };
    build(lo0, hi0, tc);
    build(lo1, hi1, tc + 128);
    if (tc < 8) *(v4u*)(fb + tc * CP_STRIDE + 256 * 16) = v4u{0u, 0u, 0u, 0u};
    if (tc < 32) *(v4u*)(ub + tc * 16) = v4u{0u, 0u, 0u, 0u};
    *(v4u*)(ub + 512 + tc * 16) = u0;
    *(v4u*)(ub + 512 + 2048 + tc * 16) = u1;
    __syncthreads();
    v4f acc[8];
#pragma unroll
    for (int n = 0; n < 8; ++n) acc[n] = v4f{0.f, 0.f, 0.f, 0.f};
    const char* ap = fb + (c & 7) * CP_STRIDE + 2 * (2024 - 8 * (c >> 3) + 8 * g - 32 * 4 * wh - 32 * 3 - 256 * 7);
    const char* bp = ub + 512 + 2 * (16 * c - 16 + 8 * g - 32 * 4 * wh - 32 * 3);
#pragma unroll
    for (int rr = 0; rr < 4; ++rr) {
      v4u A[8], B[8];
#pragma unroll
      for (int i = 0; i < 8; ++i) {
        A[i] = *(const v4u*)(ap + 2 * (32 * (3 - rr) + 256 * (7 - i)));
        B[i] = *(const v4u*)(bp + 2 * (32 * (3 - rr) + 256 * i));
      }
#pragma unroll
      for (int i = 0; i < 8; ++i)
#pragma unroll
        for (int n = i; n < 8; ++n) acc[n] = mfma16(A[i], B[n - i], acc[n]);
    }
    __syncthreads();
    v4f* xch = (v4f*)(lds) + ch * 512;
    v4f mine[4];
#pragma unroll
    for (int n = 0; n < 4; ++n) {
      v4f snd;
#pragma unroll
      for (int e = 0; e < 4; ++e) {
        float lo = acc[n][e], hi = acc[4 + n][e];
        asm volatile("" : "+v"(lo), "+v"(hi));
        snd[e] = wh ? lo : hi;
        mine[n][e] = wh ? hi : lo;
      }
      xch[wh * 256 + n * 64 + l] = snd;
    }
    __syncthreads();
    u16* x1T = (u16*)(ws + OFF_X1T) + (size_t)d * S_;
#pragma unroll
    for (int n = 0; n < 4; ++n) {
      v4f o = xch[(1 - wh) * 256 + n * 64 + l];
      v4f y = o + mine[n];
      v2u kvq = *(const v2u*)(ub + 512 + 2 * (sl + 256 * n));
      float kv[4] = {bf_lo(kvq[0]), bf_hi(kvq[0]), bf_lo(kvq[1]), bf_hi(kvq[1])};
      float xv[4] = {bf_lo(xq[n][0]), bf_hi(xq[n][0]), bf_lo(xq[n][1]), bf_hi(xq[n][1])};
      float mu[4] = {st[n][0][0], st[n][0][2], st[n][1][0], st[n][1][2]};
      float rs[4] = {st[n][0][1], st[n][0][3], st[n][1][1], st[n][1][3]};
      float r[4];
#pragma unroll
      for (int e = 0; e < 4; ++e) {
        float z = y[e] + kv[e] * dd;
        float h = (xv[e] - mu[e]) * rs[e] * ca1 + sa;
        r[e] = xv[e] + ga * (h * z);
      }
      v2u o2 = {pack2(r[0], r[1]), pack2(r[2], r[3])};
      *(v2u*)(x1T + sl + 256 * n) = o2;
    }
  } else {
    const int u = b - K2_A;
    const int dg = u & 7, fg = u >> 3;
    float* sm = (float*)lds;
    const int fp = l & 31, combo = 2 * w + (l >> 5);
    const int ks2 = combo >> 2, g = combo & 3;
    const int f = 64 * fg + 2 * fp;
    const bool act = f < F_;
    const int fc = act ? f : 0;
    u32 wv[8];
#pragma unroll
    for (int j = 0; j < 8; ++j) {
      int d = 64 * dg + 32 * ks2 + 16 * (j >> 2) + 4 * g + (j & 3);
      wv[j] = *(const u32*)(w1 + (size_t)d * F_ + fc);
    }
    if (t < 64) sm[t] = rbf(1.0f + mod_val(modp, 2048 + 64 * dg + t));
    else if (t < 128) sm[t] = mod_val(modp, 1536 + 64 * dg + (t - 64));
    __syncthreads();
    float t1a = 0.f, t1b = 0.f, t2a = 0.f, t2b = 0.f;
    u32 pa[8];
#pragma unroll
    for (int j = 0; j < 8; ++j) {
      int dl = 32 * ks2 + 16 * (j >> 2) + 4 * g + (j & 3);
      float cb1 = sm[dl], sb = sm[64 + dl];
      float wa = bf_lo(wv[j]), wb = bf_hi(wv[j]);
      u32 p = pack2(wa * cb1, wb * cb1);
      pa[j] = p;
      t1a += bf_lo(p); t1b += bf_hi(p);
      t2a += sb * wa; t2b += sb * wb;
    }
    v4u lo, hi;
#pragma unroll
    for (int i = 0; i < 4; ++i) {
      lo[i] = (pa[2 * i] & 0xffffu) | (pa[2 * i + 1] << 16);
      hi[i] = (pa[2 * i] >> 16) | (pa[2 * i + 1] & 0xffff0000u);
    }
    if (act) {
      char* dst = ws + OFF_W1T + ((size_t)((f >> 4) * 16 + 2 * dg + ks2) * 64 + g * 16 + (f & 15)) * 16;
      *(v4u*)dst = lo;
      *(v4u*)(dst + 16) = hi;
    }
    t1a = xor32_add(t1a); t1b = xor32_add(t1b); t2a = xor32_add(t2a); t2b = xor32_add(t2b);
    v4f* r4 = (v4f*)(sm + 128);
    if (l < 32) r4[w * 32 + l] = v4f{t1a, t2a, t1b, t2b};
    __syncthreads();
    if (t < 32 && act) {
      v4f s = (r4[t] + r4[32 + t]) + (r4[64 + t] + r4[96 + t]);
      if (dg == 0) { s[1] += bf1(b1[f]); s[3] += bf1(b1[f + 1]); }
      float* tp = (float*)(ws + OFF_TP);
      *(v2f*)(tp + (size_t)f * 16 + dg * 2) = v2f{s[0], s[1]};
      *(v2f*)(tp + (size_t)(f + 1) * 16 + dg * 2) = v2f{s[2], s[3]};
    }
  }
}

constexpr int K3_LDS_T = 65536;
constexpr int K3_LDS_S = K3_LDS_T + 176 * 8;
constexpr int K3_LDS = K3_LDS_S + 64 * 8;

template <int NT>
DI void k3_body(char* lds, char* __restrict__ ws, int rt, int ft0, int ftw, int nft, int w) {
  const int t = threadIdx.x, l = t & 63;
  const u16* x1T = (const u16*)(ws + OFF_X1T);
  v4u xs[16];
#pragma unroll
  for (int i = 0; i < 16; ++i) {
    int d = 32 * i + 8 * w + (l >> 3);
    xs[i] = *(const v4u*)(x1T + (size_t)d * S_ + 64 * rt + 8 * (l & 7));
  }
  v4f tq[4];
  const int tf = t < 16 * nft ? t : 0;
#pragma unroll
  for (int i = 0; i < 4; ++i) tq[i] = *(const v4f*)(ws + OFF_TP + (size_t)(16 * ft0 + tf) * 64 + 16 * i);
  const v4u* wp = (const v4u*)(ws + OFF_W1T) + (size_t)ftw * 16 * 64 + l;
  v4u A[16][NT];
#pragma unroll
  for (int ks = 0; ks < 16; ++ks)
#pragma unroll
    for (int a = 0; a < NT; ++a) A[ks][a] = wp[(a * 16 + ks) * 64];
  __builtin_amdgcn_sched_barrier(0);
  {
    const int c16 = l & 7;
    const int sw = ((8 * w + (l >> 3)) >> 1) & 3;
    char* dst = lds + (8 * w + (l >> 3)) * 128 + (((c16 >> 1) ^ sw) * 32) + (c16 & 1) * 16;
#pragma unroll
    for (int i = 0; i < 16; ++i) *(v4u*)(dst + i * 4096) = xs[i];
    v4f s = (tq[0] + tq[1]) + (tq[2] + tq[3]);
    if (t < 176) *(v2f*)(lds + K3_LDS_T + t * 8) = v2f{s[0] + s[2], s[1] + s[3]};
  }
  __syncthreads();
  const int g = l >> 4, q = (l & 15) >> 2, p = l & 3;
  const int sw = (2 * g + (q >> 1)) & 3;
  LAS char* rb[4];
#pragma unroll
  for (int i = 0; i < 4; ++i) {
    int st = (i + w) & 3;
    rb[i] = (LAS char*)lds + (4 * g + q) * 128 + ((st ^ sw) * 32) + p * 8;
  }
  v4f acc[NT][4];
#pragma unroll
  for (int a = 0; a < NT; ++a)
#pragma unroll
    for (int i = 0; i < 4; ++i) acc[a][i] = v4f{0.f, 0.f, 0.f, 0.f};
  float s1 = 0.f, s2 = 0.f;
#pragma unroll
  for (int ks = 0; ks < 16; ++ks) {
    v4u B[4];
#pragma unroll
    for (int i = 0; i < 4; ++i) {
      v2u h0 = __builtin_bit_cast(v2u, __builtin_amdgcn_ds_read_tr16_b64_v4i16((LAS v4s*)(rb[i] + ks * 4096)));
      v2u h1 = __builtin_bit_cast(v2u, __builtin_amdgcn_ds_read_tr16_b64_v4i16((LAS v4s*)(rb[i] + ks * 4096 + 2048)));
      B[i] = v4u{h0[0], h0[1], h1[0], h1[1]};
    }
#pragma unroll
    for (int e = 0; e < 4; ++e) {
      s1 = dot2(B[0][e], 0x3f803f80u, s1);
      s2 = dot2(B[0][e], B[0][e], s2);
    }
#pragma unroll
    for (int a = 0; a < NT; ++a)
#pragma unroll
      for (int i = 0; i < 4; ++i) acc[a][i] = mfma16(A[ks][a], B[i], acc[a][i]);
  }
  s1 = xor32_add(xor16_add(s1));
  s2 = xor32_add(xor16_add(s2));
  {
    float m = s1 * (1.0f / D_);
    float var = fmaxf(s2 * (1.0f / D_) - m * m, 0.f);
    v2f stt = ln_stats(m, var);
    if (l < 16) *(v2f*)(lds + K3_LDS_S + (w * 16 + l) * 8) = stt;
  }
  __syncthreads();
  char* hid = ws + OFF_HID;
#pragma unroll
  for (int i = 0; i < 4; ++i) {
    const int st = (i + w) & 3;
    const v2f stt = *(const v2f*)(lds + K3_LDS_S + (st * 16 + (l & 15)) * 8);
#pragma unroll
    for (int a = 0; a < NT; ++a) {
      const int ft = ftw + a;
      const v4f ta = *(const v4f*)(lds + K3_LDS_T + ((ft - ft0) * 16 + 4 * g) * 8);
      const v4f tb = *(const v4f*)(lds + K3_LDS_T + ((ft - ft0) * 16 + 4 * g) * 8 + 16);
      float t1v[4] = {ta[0], ta[2], tb[0], tb[2]}, t2v[4] = {ta[1], ta[3], tb[1], tb[3]};
      float r[4];
#pragma unroll
      for (int e = 0; e < 4; ++e) {
        float pre = (acc[a][i][e] - stt[0] * t1v[e]) * stt[1] + t2v[e];
        pre = fmaxf(rbf(pre), 0.f);
        r[e] = pre * pre;
      }
      v2u o = {pack2(r[0], r[1]), pack2(r[2], r[3])};
      *(v2u*)(hid + ((size_t)(4 * rt + st) * NK2 + (ft >> 1)) * 1024 + l * 16 + (ft & 1) * 8) = o;
    }
  }
}

__global__ __launch_bounds__(256, 1) void k3_gemm1(char* __restrict__ ws) {
  __shared__ __attribute__((aligned(16))) char lds[K3_LDS];
  const int b = blockIdx.x;
  const int w = __builtin_amdgcn_readfirstlane(threadIdx.x >> 6);
  const int xcd = b & 7, m = b >> 3;
  const int rt = 4 * xcd + (m >> 3), cg = m & 7;
  const int ft0 = cg < 7 ? 11 * cg : 76;
  const int nft = cg < 6 ? 11 : 10;
  const int ftw = ft0 + (w < 3 ? 3 * w : nft - 2);
  const int nt = (w == 3 || (w == 2 && nft == 10)) ? 2 : 3;
  if (nt == 3) k3_body<3>(lds, ws, rt, ft0, ftw, nft, w);
  else k3_body<2>(lds, ws, rt, ft0, ftw, nft, w);
}

constexpr int K4_LDS_X = 65536;
constexpr int K4_LDS_G = K4_LDS_X + 8192;
constexpr int K4_LDS = K4_LDS_G + 512;

template <int NKS>
DI void k4_main(const v4u* __restrict__ ap, const v4u* __restrict__ bp, v4f (&acc)[4][4]) {
  constexpr int W = 6;
  v4u A[W][4], B[W][4];
#pragma unroll
  for (int i = 0; i < W; ++i)
#pragma unroll
    for (int a = 0; a < 4; ++a) {
      A[i][a] = ap[(size_t)(i * 4 * 32 + a) * 64];
      B[i][a] = bp[(size_t)(a * NK2 + i * 4) * 64];
    }
  __builtin_amdgcn_sched_barrier(0);
#pragma unroll
  for (int i = 0; i < NKS; ++i) {
#pragma unroll
    for (int a = 0; a < 4; ++a)
#pragma unroll
      for (int s = 0; s < 4; ++s) acc[a][s] = mfma16(A[i % W][a], B[i % W][s], acc[a][s]);
    if (i + W < NKS) {
#pragma unroll
      for (int a = 0; a < 4; ++a) {
        A[i % W][a] = ap[(size_t)((i + W) * 4 * 32 + a) * 64];
        B[i % W][a] = bp[(size_t)(a * NK2 + (i + W) * 4) * 64];
      }
    }
    __builtin_amdgcn_sched_barrier(0);
  }
}

__global__ __launch_bounds__(256, 1) void k4_gemm2(const u16* __restrict__ b2, char* __restrict__ ws, u16* __restrict__ out) {
  __shared__ __attribute__((aligned(16))) char lds[K4_LDS];
  const int b = blockIdx.x, t = threadIdx.x, l = t & 63;
  const int w = __builtin_amdgcn_readfirstlane(t >> 6);
  const int xcd = b & 7, m = b >> 3;
  const int rt = 4 * xcd + (m >> 3), db = m & 7;
  const u16* x1T = (const u16*)(ws + OFF_X1T);
  v4u xs[2];
#pragma unroll
  for (int i = 0; i < 2; ++i) {
    int dl = 32 * i + 8 * w + (l >> 3);
    xs[i] = *(const v4u*)(x1T + (size_t)(64 * db + dl) * S_ + 64 * rt + 8 * (l & 7));
  }
  const float* modp = (const float*)(ws + OFF_MOD);
  float gbv = mod_val(modp, 2560 + 64 * db + l), b2v = bf1(b2[64 * db + l]);
  v4f acc[4][4];
#pragma unroll
  for (int a = 0; a < 4; ++a)
#pragma unroll
    for (int s = 0; s < 4; ++s) acc[a][s] = v4f{0.f, 0.f, 0.f, 0.f};
  const v4u* ap = (const v4u*)(ws + OFF_W2T) + (size_t)(w * 32 + 4 * db) * 64 + l;
  const v4u* bp = (const v4u*)(ws + OFF_HID) + (size_t)(4 * rt * NK2 + w) * 64 + l;
  if (w < 3) k4_main<11>(ap, bp, acc);
  else k4_main<10>(ap, bp, acc);
  {
    const int c16 = l & 7;
#pragma unroll
    for (int i = 0; i < 2; ++i) {
      int dl = 32 * i + 8 * w + (l >> 3);
      int sw = ((dl >> 1) & 1) | ((dl >> 3) & 2);
      *(v4u*)(lds + K4_LDS_X + dl * 128 + (((c16 >> 1) ^ sw) * 32) + (c16 & 1) * 16) = xs[i];
    }
    if (w == 0) *(v2f*)(lds + K4_LDS_G + l * 8) = v2f{gbv, b2v};
  }
  v4f* red = (v4f*)lds;
#pragma unroll
  for (int s = 0; s < 4; ++s)
#pragma unroll
    for (int a = 0; a < 4; ++a) red[((w * 4 + s) * 4 + a) * 64 + l] = acc[a][s];
  __syncthreads();
  const int g = l >> 4, q = (l & 15) >> 2, p = l & 3;
  u32 o[8];
#pragma unroll
  for (int a = 0; a < 4; ++a) {
    v4f v = (red[((0 * 4 + w) * 4 + a) * 64 + l] + red[((1 * 4 + w) * 4 + a) * 64 + l]) +
            (red[((2 * 4 + w) * 4 + a) * 64 + l] + red[((3 * 4 + w) * 4 + a) * 64 + l]);
    int dl = 16 * g + 4 * a + q;
    int sw = ((dl >> 1) & 1) | ((dl >> 3) & 2);
    v2u xq = __builtin_bit_cast(v2u, __builtin_amdgcn_ds_read_tr16_b64_v4i16(
                                         (LAS v4s*)((LAS char*)lds + K4_LDS_X + dl * 128 + ((w ^ sw) * 32) + p * 8)));
    const v4f ga = *(const v4f*)(lds + K4_LDS_G + (16 * g + 4 * a) * 8);
    const v4f gb = *(const v4f*)(lds + K4_LDS_G + (16 * g + 4 * a) * 8 + 16);
    float r0 = bf_lo(xq[0]) + rbf(ga[0] * rbf(rbf(v[0]) + ga[1]));
    float r1 = bf_hi(xq[0]) + rbf(ga[2] * rbf(rbf(v[1]) + ga[3]));
    float r2 = bf_lo(xq[1]) + rbf(gb[0] * rbf(rbf(v[2]) + gb[1]));
    float r3 = bf_hi(xq[1]) + rbf(gb[2] * rbf(rbf(v[3]) + gb[3]));
    o[2 * a] = pack2(r0, r1);
    o[2 * a + 1] = pack2(r2, r3);
  }
  u16* dst = out + (size_t)(64 * rt + 16 * w + (l & 15)) * D_ + 64 * db + 16 * g;
  *(v4u*)dst = v4u{o[0], o[1], o[2], o[3]};
  *(v4u*)(dst + 8) = v4u{o[4], o[5], o[6], o[7]};
}

extern "C" void kernel_launch(void* const* d_in, const int* in_sizes, int n_in, void* d_out, int out_size,
                              void* d_ws, size_t ws_size, hipStream_t stream) {
  const u16* x = (const u16*)d_in[0];
  const u16* k = (const u16*)d_in[1];
  const u16* v = (const u16*)d_in[2];
  const u16* shk = (const u16*)d_in[3];
  const u16* ssmk = (const u16*)d_in[4];
  const u16* ssmD = (const u16*)d_in[5];
  const u16* w1 = (const u16*)d_in[7];
  const u16* b1 = (const u16*)d_in[8];
  const u16* w2 = (const u16*)d_in[9];
  const u16* b2 = (const u16*)d_in[10];
  const u16* cvec = (const u16*)d_in[11];
  const u16* wmod = (const u16*)d_in[12];
  char* ws = (char*)d_ws;
  k1_prep<<<K1_A + K1_B + K1_C, 256, 0, stream>>>(x, k, v, shk, cvec, wmod, w2, ws);
  k2_conv<<<K2_A + K2_B, 256, 0, stream>>>(ssmk, ssmD, w1, b1, ws);
  k3_gemm1<<<256, 256, 0, stream>>>(ws);
  k4_gemm2<<<256, 256, 0, stream>>>(b2, ws, (u16*)d_out);
}
```

```cpp
#include <hip/hip_runtime.h>
#include <hip/hip_bf16.h>
#include <stdint.h>

typedef unsigned short u16;
typedef unsigned int u32;
typedef u32 v4u __attribute__((ext_vector_type(4)));
typedef u32 v2u __attribute__((ext_vector_type(2)));
typedef float v4f __attribute__((ext_vector_type(4)));
typedef float v2f __attribute__((ext_vector_type(2)));
typedef short v4s __attribute__((ext_vector_type(4)));
typedef __bf16 v8bf __attribute__((ext_vector_type(8)));
typedef __bf16 v2bf __attribute__((ext_vector_type(2)));
#define LAS __attribute__((address_space(3)))
#define GAS __attribute__((address_space(1)))
#define DI __device__ __forceinline__
#define SB() __builtin_amdgcn_sched_barrier(0)
#define DRAIN() asm volatile("s_waitcnt vmcnt(0)" ::: "memory")
#define LGKM0() asm volatile("s_waitcnt lgkmcnt(0)" ::: "memory")
#define WG_BARRIER() do { SB(); LGKM0(); __builtin_amdgcn_s_barrier(); asm volatile("" ::: "memory"); SB(); } while (0)

constexpr int S_ = 2048, D_ = 512, F_ = 1376;
constexpr int NK2 = F_ / 32;

constexpr int OFF_KVT = 0;
constexpr int OFF_XT  = OFF_KVT + D_ * S_ * 2;
constexpr int OFF_X1B = OFF_XT + D_ * S_ * 2;
constexpr int OFF_UB  = OFF_X1B + D_ * S_ * 2;
constexpr int OFF_HID = OFF_UB + D_ * S_ * 2;
constexpr int OFF_W1T = OFF_HID + S_ * F_ * 2;
constexpr int OFF_ST1 = OFF_W1T + D_ * F_ * 2;
constexpr int OFF_MOD = OFF_ST1 + S_ * 8;
constexpr int OFF_PST = OFF_MOD + 3072 * 16;
constexpr int OFF_FLG = OFF_PST + 32 * 4 * 8 * 256;
constexpr int FL_A = 0, FL_X = 256, FL_C = 512, FL_B = 768, FL_EPOCH = 832;
constexpr int WS_END  = OFF_FLG + 833 * 128;
constexpr int OOB = 0x7fff0000;

constexpr int L_U = 0, U_BYTES = 4096 + 512;
constexpr int L_XCH = 4 * U_BYTES;
constexpr int L_KIMG = L_XCH, L_VIMG = L_XCH + 68 * 128;
constexpr int L_OPX = L_XCH + 17408;
constexpr int L_OPH = L_OPX + 8192;
constexpr int L_MRED = L_OPH + 16384;
constexpr int L_W1C = 65536;
constexpr int L_CSC = L_W1C + 49152;
constexpr int CP_STRIDE = 4160, CH_FILT = 8 * CP_STRIDE;
constexpr int L_FILT = 65536;
constexpr int L_TB = L_FILT + 2 * CH_FILT;
constexpr int L_TT = L_TB + 2048;
constexpr int L_ST2 = L_TT + 1536;
constexpr int L_GB = L_ST2 + 512;
constexpr int L_XS = L_GB + 512;
constexpr int L_GSC = L_XS + 8192;
constexpr int L_SYNC = L_GSC + 16384;
constexpr int LDS_BYTES = L_SYNC + 128;
static_assert(L_MRED + 2048 <= 65536 && LDS_BYTES <= 163840, "lds");
enum { SY_KV, SY_A1CNT, SY_A1GO, SY_PAIR0, SY_PAIR1, SY_OPS, SY_BCNT, SY_MOD, SY_ST2, SY_CCNT, SY_CGO, SY_FB0, SY_FB1, SY_ARD, SY_TAB, SY_WCNT, SY_XI, SY_XCNT, SY_XGO, SY_BGO, SY_WGO, SY_C1 };

#define GDELAY 8
#define FLATE 1
#define CPRIO 2
#define W2E 7
#define W1A 0

#define STAMP(i)
#define STAMP_FLUSH()
#define TS_DECL

DI int lane_id() {
  int l = __builtin_amdgcn_mbcnt_hi(~0u, __builtin_amdgcn_mbcnt_lo(~0u, 0u));
  asm volatile("" : "+v"(l));
  __builtin_assume(l >= 0 && l < 64);
  return l;
}
DI float bf_lo(u32 w) { return __builtin_bit_cast(float, w << 16); }
DI float bf_hi(u32 w) { return __builtin_bit_cast(float, w & 0xffff0000u); }
DI float bf1(u16 h) { return __builtin_bit_cast(float, (u32)h << 16); }
DI u32 pack2(float a, float b) {
  v2f f = {a, b};
  return __builtin_bit_cast(u32, __builtin_convertvector(f, v2bf));
}
DI float rbf(float a) { return bf_lo(pack2(a, a)); }
DI v4f mfma16(v4u a, v4u b, v4f c) {
  return __builtin_amdgcn_mfma_f32_16x16x32_bf16(__builtin_bit_cast(v8bf, a), __builtin_bit_cast(v8bf, b), c, 0, 0, 0);
}
template <int CTRL> DI float dpp_mov(float v) {
  return __builtin_bit_cast(float, __builtin_amdgcn_update_dpp(0, __builtin_bit_cast(int, v), CTRL, 0xf, 0xf, true));
}
DI float xor16_add(float v) {
  u32 u = __builtin_bit_cast(u32, v);
  auto r = __builtin_amdgcn_permlane16_swap(u, u, false, false);
  return __builtin_bit_cast(float, (u32)r[0]) + __builtin_bit_cast(float, (u32)r[1]);
}
DI float xor32_add(float v) {
  u32 u = __builtin_bit_cast(u32, v);
  auto r = __builtin_amdgcn_permlane32_swap(u, u, false, false);
  return __builtin_bit_cast(float, (u32)r[0]) + __builtin_bit_cast(float, (u32)r[1]);
}
DI float wave_sum(float v) {
  v += dpp_mov<0xB1>(v);
  v += dpp_mov<0x4E>(v);
  v += dpp_mov<0x141>(v);
  v += dpp_mov<0x140>(v);
  v = xor16_add(v);
  v = xor32_add(v);
  return v;
}
DI float dot2(u32 a, u32 b, float c) {
  return __builtin_amdgcn_fdot2_f32_bf16(__builtin_bit_cast(v2bf, a), __builtin_bit_cast(v2bf, b), c, false);
}
DI u32 ln_stats(float mean, float var) { return pack2(mean, __builtin_amdgcn_sqrtf(rbf(rbf(var) + 1e-5f))); }
DI v2u tr_read(LAS char* p) { return __builtin_bit_cast(v2u, __builtin_amdgcn_ds_read_tr16_b64_v4i16((LAS v4s*)p)); }
DI float as_f(u32 u) { return __builtin_bit_cast(float, u); }
DI u32 as_u(float f) { return __builtin_bit_cast(u32, f); }
DI float sum2r(v4u p) { return rbf(as_f(p[0]) + as_f(p[2])); }
DI u32 tagdiff(v4u p, u32 tag) { return (p[1] ^ tag) | (p[3] ^ tag); }

typedef __amdgpu_buffer_rsrc_t rsrc_t;
constexpr int SC1 = 16;
DI v4u ld16(rsrc_t r, int off, int soff) { return __builtin_amdgcn_raw_buffer_load_b128(r, off, soff, SC1); }
DI v2u ld8(rsrc_t r, int off, int soff) { return __builtin_amdgcn_raw_buffer_load_b64(r, off, soff, SC1); }
DI void st16(rsrc_t r, int off, int soff, v4u v) { __builtin_amdgcn_raw_buffer_store_b128(v, r, (int)((u32)off + (u32)soff), 0, SC1); }
DI void st8(rsrc_t r, int off, int soff, v2u v) { __builtin_amdgcn_raw_buffer_store_b64(v, r, off, soff, SC1); }
DI void st4(rsrc_t r, int off, int soff, u32 v) { __builtin_amdgcn_raw_buffer_store_b32(v, r, off, soff, SC1); }

constexpr int SPIN_CAP = 1 << 20;
DI GAS u32* flag_ptr(char* ws, int idx) { return (GAS u32*)(ws + OFF_FLG + idx * 128); }
template <int SLEEP> DI void poll_flags(GAS u32* f, bool act, u32 tag) {
  bool pend = act;
#pragma nounroll
  for (int spin = 0; spin < SPIN_CAP; ++spin) {
    if (pend) pend = __hip_atomic_load(f, __ATOMIC_RELAXED, __HIP_MEMORY_SCOPE_AGENT) != tag;
    if (!__any(pend)) break;
    __builtin_amdgcn_s_sleep(SLEEP);
  }
  asm volatile("" ::: "memory");
}
DI void lds_arrive(LAS u32* c, int l) {
  LGKM0();
  if (l == 0) __hip_atomic_fetch_add(c, 1u, __ATOMIC_RELAXED, __HIP_MEMORY_SCOPE_WORKGROUP);
}
DI void lds_wait(LAS u32* c, u32 n) {
#pragma nounroll
  for (int spin = 0; spin < (SPIN_CAP << 4); ++spin) {
    if (__hip_atomic_load(c, __ATOMIC_RELAXED, __HIP_MEMORY_SCOPE_WORKGROUP) >= n) break;
    __builtin_amdgcn_s_sleep(1);
  }
  asm volatile("" ::: "memory");
}
template <int N> DI void publish(LAS u32* c, GAS u32* f, u32 tag, int l) {
  DRAIN();
  if (l == 0) {
    u32 old = __hip_atomic_fetch_add(c, 1u, __ATOMIC_RELAXED, __HIP_MEMORY_SCOPE_WORKGROUP);
    if (old == N - 1) __hip_atomic_store(f, tag, __ATOMIC_RELAXED, __HIP_MEMORY_SCOPE_AGENT);
  }
}
DI u32 get_tag(u32 ep) {
  u32 tag = __builtin_amdgcn_readfirstlane(ep) + 1u;
  asm volatile("" : "+s"(tag));
  return tag;
}
DI int oct_sw(int row) { return ((row >> 1) & 1) | ((row >> 2) & 2); }

DI void w2_transpose(LAS char* sc, v4u (&W)[4], int l) {
  const int r8 = l >> 3, c8 = l & 7, g = l >> 4, q = (l & 15) >> 2, p = l & 3;
  LAS char* wp = sc + r8 * 128 + ((c8 ^ ((r8 & 2) << 1)) * 16);
#pragma unroll
  for (int i = 0; i < 4; ++i) *(LAS v4u*)(wp + i * 1024) = W[i];
  LAS char* rp = sc + (4 * g + q) * 128 + 16 * p;
#pragma unroll
  for (int a = 0; a < 4; ++a) {
    const int co = (((a >> 1) ^ (q >> 1)) * 64) + 8 * (a & 1);
    const v2u h0 = tr_read(rp + co), h1 = tr_read(rp + co + 2048);
    W[a] = v4u{h0[0], h0[1], h1[0], h1[1]};
  }
}
DI u32 combine_stats(v4u pa, v4u pc) {
  const float m = xor32_add(xor16_add(as_f(pa[0]) + as_f(pc[0]))) * (1.0f / D_);
  const float da = as_f(pa[0]) * (1.0f / 64) - m, dc = as_f(pc[0]) * (1.0f / 64) - m;
  const float m2 = xor32_add(xor16_add((as_f(pa[2]) + as_f(pc[2])) + 64.f * (da * da + dc * dc)));
  return ln_stats(m, m2 * (1.0f / D_));
}
DI void wait_partials(rsrc_t rs, int pb, int l, u32 tag, v4u& pa, v4u& pc) {
#pragma nounroll
  for (int spin = 0; spin < SPIN_CAP; ++spin) {
    asm volatile("" ::: "memory");
    pa = ld16(rs, l * 16, pb);
    pc = ld16(rs, l * 16 + 1024, pb);
    if (!__any((tagdiff(pa, tag) | tagdiff(pc, tag)) != 0u)) break;
    __builtin_amdgcn_s_sleep(2);
  }
}
DI float sqdev(u32 x, float m, float acc) {
  const float e0 = bf_lo(x) - m, e1 = bf_hi(x) - m;
  return acc + e0 * e0 + e1 * e1;
}
DI v2f lo2(v4f v) { return __builtin_shufflevector(v, v, 0, 1); }
DI v2f hi2(v4f v) { return __builtin_shufflevector(v, v, 2, 3); }
DI v2f fma2(v2f a, v2f b, v2f c) { return __builtin_elementwise_fma(a, b, c); }
DI v2f up2(u32 w) { return v2f{bf_lo(w), bf_hi(w)}; }
DI u32 pk2(v2f v) { return __builtin_bit_cast(u32, __builtin_convertvector(v, v2bf)); }
DI v2u act4(v4f acc, float rs, float mr, v4f t1, v4f t2) {
  const v2f r2 = {rs, rs}, m2 = {mr, mr};
  v2f p0 = fma2(lo2(acc), r2, fma2(lo2(t1), m2, lo2(t2))), p1 = fma2(hi2(acc), r2, fma2(hi2(t1), m2, hi2(t2)));
  p0 = v2f{fmaxf(p0[0], 0.f), fmaxf(p0[1], 0.f)};
  p1 = v2f{fmaxf(p1[0], 0.f), fmaxf(p1[1], 0.f)};
  return v2u{pk2(p0 * p0), pk2(p1 * p1)};
}
DI void stage_u(LAS char* lds, rsrc_t rs, int rt, int w, int l) {
  v4u xs[8];
#pragma unroll
  for (int i = 0; i < 8; ++i) xs[i] = ld16(rs, l * 16 + (i & 3) * 1024, OFF_UB + rt * 65536 + w * 8192 + (i >> 2) * 4096);
  const int c16 = l & 7, r8 = l >> 3;
  LAS char* dst = lds + w * 8192 + r8 * 128 + (((c16 >> 1) ^ ((r8 >> 1) & 3)) * 32) + (c16 & 1) * 16;
#pragma unroll
  for (int i = 0; i < 8; ++i) *(LAS v4u*)(dst + i * 1024) = xs[i];
}
DI void dump_acc(LAS char* dst, const v4f (&acc)[4][4], int l) {
#pragma unroll
  for (int s = 0; s < 4; ++s)
#pragma unroll
    for (int a = 0; a < 4; ++a) *(LAS v4f*)(dst + ((s * 4 + a) * 64 + l) * 16) = acc[a][s];
}
DI void final_epilogue(LAS char* lds, u16* __restrict__ out, int rt, int db, int w, int l) {
  const int st = w >> 1, hf = w & 1, g = l >> 4, q = (l & 15) >> 2, p = l & 3;
  u32 o[4];
#pragma unroll
  for (int a = 0; a < 2; ++a) {
    v4f v[8];
#pragma unroll
    for (int k = 0; k < 8; ++k) v[k] = *(LAS v4f*)(lds + k * 16384 + ((st * 4 + 2 * hf + a) * 64 + l) * 16);
    const int dl = 32 * hf + 8 * g + 4 * a + q;
    const v2u xq = tr_read(lds + L_XS + dl * 128 + ((st ^ oct_sw(dl)) * 32) + p * 8);
    const v4f gb = *(LAS v4f*)(lds + L_GB + (32 * hf + 8 * g + 4 * a) * 4);
    const v4f bb = *(LAS v4f*)(lds + L_GB + 256 + (32 * hf + 8 * g + 4 * a) * 4);
    const v4f s = ((v[0] + v[1]) + (v[2] + v[3])) + ((v[4] + v[5]) + (v[6] + v[7]));
    u32 p0 = pk2(lo2(s)), p1 = pk2(hi2(s));
    p0 = pk2(up2(p0) + lo2(bb));
    p1 = pk2(up2(p1) + hi2(bb));
    p0 = pk2(up2(p0) * lo2(gb));
    p1 = pk2(up2(p1) * hi2(gb));
    o[2 * a] = pk2(up2(xq[0]) + up2(p0));
    o[2 * a + 1] = pk2(up2(xq[1]) + up2(p1));
  }
  *(v4u*)(out + (size_t)(64 * rt + 16 * st + (l & 15)) * D_ + 64 * db + 32 * hf + 8 * g) = v4u{o[0], o[1], o[2], o[3]};
}

__global__ __launch_bounds__(512, 2) void fused_block(const u16* __restrict__ x, const u16* __restrict__ k, const u16* __restrict__ v,
                                                      const u16* __restrict__ shk, const u16* __restrict__ ssmk,
                                                      const u16* __restrict__ ssmD, const u16* __restrict__ w1,
                                                      const u16* __restrict__ b1, const u16* __restrict__ w2,
                                                      const u16* __restrict__ b2, const u16* __restrict__ cvec,
                                                      const u16* __restrict__ wmod, char* ws, u16* __restrict__ out) {
  __shared__ __attribute__((aligned(16))) char lds_raw[LDS_BYTES];
  LAS char* lds = (LAS char*)lds_raw;
  LAS u32* sy = (LAS u32*)(lds + L_SYNC);
  const int b = blockIdx.x;
  const int w = __builtin_amdgcn_readfirstlane(threadIdx.x >> 6), wq = w & 3;
  const int grp = b & 7, j = b >> 3;
  const int rt = 8 * (grp >> 1) + (j >> 2), cg = 4 * (grp & 1) + (j & 3);
  const int nsg = cg < 6 ? 3 : 2, sg0 = cg < 6 ? 3 * cg : 2 * cg + 6;
  const rsrc_t rs = __builtin_amdgcn_make_buffer_rsrc(ws, 0, WS_END, 0x00020000);
  const rsrc_t rw2 = __builtin_amdgcn_make_buffer_rsrc((void*)w2, 0, F_ * D_ * 2, 0x00020000);
  asm volatile("" :: "s"(x), "s"(k), "s"(v), "s"(shk), "s"(ssmk), "s"(ssmD), "s"(w1), "s"(b1), "s"(w2), "s"(b2), "s"(cvec),
               "s"(wmod), "s"(ws), "s"(out));

  if (w < 4) {
    const int l = lane_id(), t = 64 * w + l;
    TS_DECL
    STAMP(0);
    __builtin_amdgcn_s_setprio(CPRIO);
    const int ch = wq >> 1, wh = wq & 1;
    const int d = 64 * grp + 2 * j + ch;
    const int c = l & 15, g = l >> 4, q = c >> 2, p = l & 3;
    const int rw = t >> 3, c8 = t & 7;
    const u32 toff = (u32)(64 * j + rw) * (D_ * 2) + (u32)(64 * grp + 8 * c8) * 2;
    const v4u k0 = *(const v4u*)((const char*)k + toff), k1 = *(const v4u*)((const char*)k + toff + 32 * D_ * 2);
    const v4u v0 = *(const v4u*)((const char*)v + toff), v1 = *(const v4u*)((const char*)v + toff + 32 * D_ * 2);
    v4u kh = *(const v4u*)((const char*)k + (j ? (u32)(64 * j - 4 + (rw & 3)) * (D_ * 2) + (u32)(64 * grp + 8 * c8) * 2 : 0u));
    v2u skq = *(const v2u*)(shk + (64 * grp + 16 * wq + c) * 4);
    SB();
    const u16* hf = ssmk + (size_t)d * S_;
    const int tc = 64 * wh + l;
    v4u hi0, lo0, hi1, lo1;
    auto fload = [&]() {
      hi0 = *(const v4u*)(hf + 2040 - 8 * tc); lo0 = *(const v4u*)(hf + 2032 - 8 * tc);
      hi1 = *(const v4u*)(hf + 1016 - 8 * tc); lo1 = *(const v4u*)(hf + (tc == 127 ? 0 : 1008 - 8 * tc));
    };
    if (!FLATE) fload();
    u32 ep = __builtin_amdgcn_raw_buffer_load_b32(rs, OFF_FLG + FL_EPOCH * 128, 0, 0);
    SB();
    if (t < 32) sy[t] = 0u;
    if (l < 32) *(LAS v4u*)(lds + L_U + wq * U_BYTES + l * 16) = v4u{0u, 0u, 0u, 0u};
    WG_BARRIER();
    {
      const int cs = (c8 & 1) * 16;
      const int o0 = rw * 128 + (((c8 >> 1) ^ oct_sw(rw)) * 32) + cs;
      *(LAS v4u*)(lds + L_KIMG + (rw + 4) * 128 + (((c8 >> 1) ^ oct_sw(rw + 4)) * 32) + cs) = k0;
      *(LAS v4u*)(lds + L_KIMG + (rw + 36) * 128 + (((c8 >> 1) ^ oct_sw(rw + 4)) * 32) + cs) = k1;
      *(LAS v4u*)(lds + L_VIMG + o0) = v0;
      *(LAS v4u*)(lds + L_VIMG + o0 + 4096) = v1;
      if (j == 0) kh = v4u{0u, 0u, 0u, 0u};
      if (t < 32) *(LAS v4u*)(lds + L_KIMG + o0) = kh;
      asm volatile("" : "+v"(ep), "+v"(skq));
      SB();
      if (FLATE) fload();
      SB();
      lds_arrive(sy + SY_KV, l);
      STAMP(13);
      lds_wait(sy + SY_KV, 4);
      const float a0 = bf_lo(skq[0]), a1 = bf_hi(skq[0]), a2 = bf_lo(skq[1]), a3 = bf_hi(skq[1]);
      const int r0 = 8 * g + q;
      const int ro = r0 * 128 + ((wq ^ oct_sw(r0)) * 32) + p * 8;
      LAS char* kp1 = lds + L_KIMG + (r0 + 8) * 128 + ((wq ^ oct_sw(r0 + 8)) * 32) + p * 8;
      const int so = (64 * grp + 16 * wq) * 4096 + j * 128;
#pragma unroll
      for (int u = 0; u < 2; ++u) {
        const v2u ka = tr_read(lds + L_KIMG + ro + u * 4096), kb = tr_read(lds + L_KIMG + ro + u * 4096 + 512), kc = tr_read(kp1 + u * 4096);
        const v2u va = tr_read(lds + L_VIMG + ro + u * 4096), vb = tr_read(lds + L_VIMG + ro + u * 4096 + 512);
        const float kf[12] = {0.f, bf_hi(ka[0]), bf_lo(ka[1]), bf_hi(ka[1]), bf_lo(kb[0]), bf_hi(kb[0]), bf_lo(kb[1]), bf_hi(kb[1]),
                              bf_lo(kc[0]), bf_hi(kc[0]), bf_lo(kc[1]), bf_hi(kc[1])};
        const u32 vw[4] = {va[0], va[1], vb[0], vb[1]};
        v4u o;
#pragma unroll
        for (int i = 0; i < 4; ++i) {
          float e0 = kf[2 * i + 1] * a3 + kf[2 * i + 2] * a2 + kf[2 * i + 3] * a1 + kf[2 * i + 4] * a0;
          float e1 = kf[2 * i + 2] * a3 + kf[2 * i + 3] * a2 + kf[2 * i + 4] * a1 + kf[2 * i + 5] * a0;
          u32 pk = pack2(e0, e1);
          o[i] = pack2(bf_lo(pk) * bf_lo(vw[i]), bf_hi(pk) * bf_hi(vw[i]));
        }
        st16(rs, c * 4096 + g * 16 + u * 64, OFF_KVT + so, o);
      }
    }
    STAMP(1);
    SB();
    const u32 tag = get_tag(ep);
    LAS char* fb = lds + L_FILT + ch * CH_FILT;
    {
      if (tc == 127) lo1 = v4u{0u, 0u, 0u, 0u};
      auto build = [&](v4u lo, v4u hi, int cidx) {
        const u32 wd[8] = {lo[0], lo[1], lo[2], lo[3], hi[0], hi[1], hi[2], hi[3]};
        u32 R[16];
#pragma unroll
        for (int n = 2; n < 16; ++n) {
          if (n & 1) R[n] = __builtin_amdgcn_alignbit(wd[n >> 1], wd[n >> 1], 16);
          else R[n] = (wd[n >> 1] & 0xffffu) | (wd[(n >> 1) - 1] & 0xffff0000u);
        }
#pragma unroll
        for (int qq = 0; qq < 8; ++qq) *(LAS v4u*)(fb + qq * CP_STRIDE + cidx * 16) = v4u{R[8 + qq], R[6 + qq], R[4 + qq], R[2 + qq]};
      };
      build(lo0, hi0, tc);
      SB();
      publish<4>(sy + SY_A1CNT, flag_ptr(ws, FL_A + grp * 32 + j), tag, l);
      STAMP(2);
      SB();
      build(lo1, hi1, tc + 128);
      if (tc < 8) *(LAS v4u*)(fb + tc * CP_STRIDE + 256 * 16) = v4u{0u, 0u, 0u, 0u};
      lds_arrive(sy + SY_FB0 + ch, l);
    }
    lds_wait(sy + SY_FB0 + ch, 2);
    v4u A[4][8];
    {
      LAS char* ap = fb + (c & 7) * CP_STRIDE + 2 * (136 - 8 * (c >> 3) + 8 * g - 128 * wh);
#pragma unroll
      for (int rr = 0; rr < 4; ++rr)
#pragma unroll
        for (int ip = 0; ip < 8; ++ip) A[rr][ip] = *(LAS v4u*)(ap + 2 * (32 * (3 - rr) + 256 * (7 - ip)));
      SB();
    }
    const float dd = bf1(ssmD[d]);
    if (wq == 0) {
      poll_flags<1>(flag_ptr(ws, FL_A + grp * 32 + (l & 31)), l < 32, tag);
      lds_arrive(sy + SY_A1GO, l);
      lds_arrive(sy + SY_ARD, l);
    } else {
      lds_arrive(sy + SY_ARD, l);
      lds_wait(sy + SY_A1GO, 1);
    }
    STAMP(3);
    LAS char* ub = lds + L_U + wq * U_BYTES;
    {
      v4u u[4];
#pragma unroll
      for (int i = 0; i < 4; ++i) u[i] = ld16(rs, l * 16 + i * 1024, OFF_KVT + d * 4096);
#pragma unroll
      for (int i = 0; i < 4; ++i) *(LAS v4u*)(ub + 512 + i * 1024 + l * 16) = u[i];
    }
    STAMP(4);
    v4f acc[8];
#pragma unroll
    for (int n = 0; n < 8; ++n) acc[n] = v4f{0.f, 0.f, 0.f, 0.f};
    {
      LAS char* bp = ub + 512 + 2 * (16 * c - 112 + 8 * g - 128 * wh);
      v4u B[2][8];
#pragma unroll
      for (int n = 0; n < 8; ++n) B[0][n] = *(LAS v4u*)(bp + 2 * (96 + 256 * n));
#pragma unroll
      for (int rr = 0; rr < 4; ++rr) {
        if (rr < 3) {
#pragma unroll
          for (int n = 0; n < 8; ++n) B[(rr + 1) & 1][n] = *(LAS v4u*)(bp + 2 * (32 * (2 - rr) + 256 * n));
        }
        SB();
#pragma unroll
        for (int n = 0; n < 8; ++n)
#pragma unroll
          for (int ip = 0; ip + n < 8; ++ip) acc[n + ip] = mfma16(A[rr][ip], B[rr & 1][n], acc[n + ip]);
        SB();
      }
    }
    STAMP(5);
    v4f mine[4];
#pragma unroll
    for (int n = 0; n < 4; ++n) {
      v4f snd;
#pragma unroll
      for (int e = 0; e < 4; ++e) {
        float lo = acc[n][e], hi = acc[4 + n][e];
        asm volatile("" : "+v"(lo), "+v"(hi));
        snd[e] = wh ? lo : hi;
        mine[n][e] = wh ? hi : lo;
      }
      *(LAS v4f*)(lds + L_XCH + wq * 4096 + (n * 64 + l) * 16) = snd;
    }
    lds_arrive(sy + SY_PAIR0 + ch, l);
    lds_wait(sy + SY_OPS, 4);
    {
      const float c1 = as_f(sy[SY_C1 + ch]);
      const int sl = 1024 * wh + 16 * c + 4 * g;
      const int vo = (c >> 2) * 65536 + ((c & 3) * 16 + 4 * g) * 2;
      v2u xq[4];
      v4f hg[4];
#pragma unroll
      for (int n = 0; n < 4; ++n) {
        const int s = sl + 256 * n;
        const v2u kq = *(LAS v2u*)(ub + 512 + 2 * s);
        xq[n] = *(LAS v2u*)(lds + L_OPX + ch * 4096 + s * 2);
        hg[n] = *(LAS v4f*)(lds + L_OPH + ch * 8192 + s * 4);
        mine[n] += v4f{bf_lo(kq[0]), bf_hi(kq[0]), bf_lo(kq[1]), bf_hi(kq[1])} * dd;
      }
      lds_wait(sy + SY_PAIR0 + ch, 2);
      STAMP(6);
#pragma unroll
      for (int n = 0; n < 4; ++n) {
        const v4f y = *(LAS v4f*)(lds + L_XCH + (wq ^ 1) * 4096 + (n * 64 + l) * 16) + mine[n];
        const u32 p0 = pk2(fma2(lo2(hg[n]), lo2(y), up2(xq[n][0]))), p1 = pk2(fma2(hi2(hg[n]), hi2(y), up2(xq[n][1])));
        const int so = (16 * wh + 4 * n) * 65536 + d * 128;
        st8(rs, vo, OFF_UB + so, v2u{pk2(up2(p0) * c1), pk2(up2(p1) * c1)});
        st8(rs, vo, OFF_X1B + so, v2u{p0, p1});
      }
    }
    STAMP(14);
    {
      DRAIN();
      u32 old = 0;
      if (l == 0) old = __hip_atomic_fetch_add(sy + SY_BCNT, 1u, __ATOMIC_RELAXED, __HIP_MEMORY_SCOPE_WORKGROUP);
      if (__builtin_amdgcn_readfirstlane(old) == 3 && l < 8)
        __hip_atomic_fetch_add(flag_ptr(ws, FL_B + l * 8 + grp), 1u, __ATOMIC_RELAXED, __HIP_MEMORY_SCOPE_AGENT);
    }
    STAMP(7);
    v4u W[7][4];
    const int wo = (l >> 3) * 1024 + (l & 7) * 16;
#pragma unroll
    for (int i = 0; i < W2E; ++i)
#pragma unroll
      for (int a = 0; a < 4; ++a)
        W[i][a] = __builtin_amdgcn_raw_buffer_load_b128(rw2, wo, (32 * (7 * wq + i) + 8 * a) * 1024 + cg * 128, 0);
    SB();
    lds_wait(sy + SY_ARD, 4);
#pragma unroll
    for (int i = 0; i < W2E; ++i) w2_transpose(lds + L_CSC + wq * 4096, W[i], l);
    SB();
    lds_wait(sy + SY_BGO, 1);
    STAMP(8);
    stage_u(lds, rs, rt, w, l);
    STAMP(9);
    WG_BARRIER();
    __builtin_amdgcn_s_setprio(0);
    if (b == 0 && t == 0) *(u32*)(ws + OFF_FLG + FL_EPOCH * 128) = tag;
    const int pb = OFF_PST + (rt * 4 + wq) * 2048;
    {
      float s1 = 0.f, s2 = 0.f;
      v2u xv[4];
#pragma unroll
      for (int i = 0; i < 4; ++i) {
        const int dl = 32 * (i >> 1) + 8 * g + 4 * (i & 1) + q;
        xv[i] = tr_read(lds + L_XS + dl * 128 + ((wq ^ oct_sw(dl)) * 32) + p * 8);
        s1 = dot2(xv[i][0], 0x3f803f80u, s1);
        s1 = dot2(xv[i][1], 0x3f803f80u, s1);
      }
      s1 = xor32_add(xor16_add(s1));
      const float mt = s1 * (1.0f / 64);
      float s3 = 0.f;
#pragma unroll
      for (int i = 0; i < 4; ++i) { s2 = sqdev(xv[i][0], mt, s2); s3 = sqdev(xv[i][1], mt, s3); }
      s2 = xor32_add(xor16_add(s2 + s3));
      if (l < 16) st16(rs, l * 16, pb + cg * 256, v4u{as_u(s1), tag, as_u(s2), tag});
    }
    v4f acc1[3];
    {
#pragma unroll
      for (int a = 0; a < 3; ++a) acc1[a] = v4f{0.f, 0.f, 0.f, 0.f};
      LAS char* rb = lds + (4 * g + q) * 128 + ((wq ^ ((2 * g + (q >> 1)) & 3)) * 32) + p * 8;
      LAS char* ra = lds + L_W1C + l * 16;
      v4u B[2], A1[2][3];
      auto fetch = [&](int ks) {
        const v2u h0 = tr_read(rb + ks * 4096), h1 = tr_read(rb + ks * 4096 + 2048);
        B[ks & 1] = v4u{h0[0], h0[1], h1[0], h1[1]};
#pragma unroll
        for (int a = 0; a < 3; ++a) A1[ks & 1][a] = *(LAS v4u*)(ra + a * 16384 + ks * 1024);
      };
      fetch(0);
#pragma unroll
      for (int ks = 0; ks < 16; ++ks) {
        if (ks < 15) fetch(ks + 1);
        if (W2E + (ks >> 1) < 7) {
#pragma unroll
          for (int i = 2 * (ks & 1); i < 2 * (ks & 1) + 2; ++i)
            W[W2E + (ks >> 1)][i] =
                __builtin_amdgcn_raw_buffer_load_b128(rw2, wo, (32 * (7 * wq + W2E + (ks >> 1)) + 8 * i) * 1024 + cg * 128, 0);
        }
        SB();
#pragma unroll
        for (int a = 0; a < 3; ++a) acc1[a] = mfma16(A1[ks & 1][a], B[ks & 1], acc1[a]);
        SB();
      }
    }
    STAMP(15);
    {
      lds_wait(sy + SY_ST2, 4);
      const v2f stt = *(LAS v2f*)(lds + L_ST2 + (wq * 16 + c) * 8);
      const int base = OFF_HID + ((4 * rt + wq) * NK2 + 32) * 1024 + sg0 * 512;
#pragma unroll
      for (int a = 0; a < 3; ++a) {
        const v4f t1 = *(LAS v4f*)(lds + L_TT + (8 + a) * 128 + 16 * g), t2 = *(LAS v4f*)(lds + L_TT + (8 + a) * 128 + 64 + 16 * g);
        st8(rs, a < nsg ? l * 8 : OOB, base + a * 512, act4(acc1[a], stt[0], stt[1], t1, t2));
      }
    }
    publish<8>(sy + SY_CCNT, flag_ptr(ws, FL_C + rt * 8 + cg), tag, l);
#pragma unroll
    for (int i = W2E; i < 7; ++i) w2_transpose(lds + L_CSC + wq * 4096, W[i], l);
    SB();
    if (wq == 0) {
      lds_wait(sy + SY_CCNT, 8);
      poll_flags<1>(flag_ptr(ws, FL_C + rt * 8 + (l & 7)), l < 8, tag);
      lds_arrive(sy + SY_CGO, l);
    } else {
      lds_arrive(sy + SY_CGO, l);
    }
    lds_wait(sy + SY_CGO, 4);
    STAMP(10);
    v4f acc2[4][4];
#pragma unroll
    for (int a = 0; a < 4; ++a)
#pragma unroll
      for (int s = 0; s < 4; ++s) acc2[a][s] = v4f{0.f, 0.f, 0.f, 0.f};
    constexpr int HW = 3;
    v4u H[7][4];
#pragma unroll
    for (int i = 0; i < HW; ++i)
#pragma unroll
      for (int s = 0; s < 4; ++s) H[i][s] = ld16(rs, l * 16, OFF_HID + ((4 * rt + s) * NK2 + 7 * wq + i) * 1024);
    SB();
#pragma unroll
    for (int i = 0; i < 7; ++i) {
#pragma unroll
      for (int s = 0; s < 4; ++s)
#pragma unroll
        for (int a = 0; a < 4; ++a) acc2[a][s] = mfma16(W[i][a], H[i][s], acc2[a][s]);
#pragma unroll
      for (int n = HW + 2 * i; n < HW + 2 * i + 2; ++n)
        if (n < 7) {
#pragma unroll
          for (int s = 0; s < 4; ++s) H[n][s] = ld16(rs, l * 16, OFF_HID + ((4 * rt + s) * NK2 + 7 * wq + n) * 1024);
        }
      SB();
    }
    dump_acc(lds + w * 16384, acc2, l);
    STAMP(11);
    WG_BARRIER();
    final_epilogue(lds, out, rt, cg, w, l);
    STAMP(12);
    STAMP_FLUSH();
  } else {
    const int l = lane_id(), tt = 64 * wq + l;
    TS_DECL
    STAMP(0);
    const int g = l >> 4, q = (l & 15) >> 2, p = l & 3;
    const int rw = tt >> 3, c8 = tt & 7;
    const u32 toff = (u32)(64 * j + rw) * (D_ * 2) + (u32)(64 * grp + 8 * c8) * 2;
    u32 ep = __builtin_amdgcn_raw_buffer_load_b32(rs, OFF_FLG + FL_EPOCH * 128, 0, 0);
    u32 bbase = __hip_atomic_load(flag_ptr(ws, FL_B + grp * 8 + (l & 7)), __ATOMIC_RELAXED, __HIP_MEMORY_SCOPE_AGENT);
    WG_BARRIER();
    __builtin_amdgcn_s_sleep(GDELAY);
    const v4u x0 = *(const v4u*)((const char*)x + toff), x1 = *(const v4u*)((const char*)x + toff + 32 * D_ * 2);
    const int r0 = 8 * b + 2 * wq;
    const v4u xa = *(const v4u*)(x + (size_t)r0 * D_ + l * 8);
    const v4u xb = *(const v4u*)(x + (size_t)(r0 + 1) * D_ + l * 8);
    const bool do_mod = b >= 64;
    const int mr_ = j - 8, mcg = 2 * (grp * 6 + (mr_ >> 2)) + (mr_ & 1), mks = (mr_ >> 1) & 1;
    const int tk = 4 * (b & 3) + wq;
    v4u tv[2][4];
    u32 cc[4];
    {
      const rsrc_t rsd = __builtin_amdgcn_make_buffer_rsrc((void*)(do_mod ? wmod : w1), 0, do_mod ? 512 * 6144 : F_ * D_ * 2, 0x00020000);
      const int col = 64 * (b >> 2) + 8 * (l & 7);
      const int o0 = do_mod ? (tt >> 2) * 6144 + (tt & 3) * 16 : (l >> 3) * (F_ * 2) + col * 2;
      const int o1 = (!do_mod && col + 1024 < F_) ? o0 + 2048 : OOB;
      const int sb = do_mod ? mks * 256 * 6144 + mcg * 64 : 32 * tk * (F_ * 2), ss = do_mod ? 64 * 6144 : 8 * F_ * 2;
#pragma unroll
      for (int i = 0; i < 4; ++i) tv[0][i] = __builtin_amdgcn_raw_buffer_load_b128(rsd, o0, sb + i * ss, 0);
#pragma unroll
      for (int i = 0; i < 4; ++i) tv[1][i] = __builtin_amdgcn_raw_buffer_load_b128(rsd, o1, sb + i * ss, 0);
#pragma unroll
      for (int i = 0; i < 4; ++i) cc[i] = cvec[(mks & 1) * 256 + (tt >> 2) + 64 * i];
    }
    u32 wpf = __builtin_amdgcn_raw_buffer_load_b32(rw2, tt < 43 ? (b * 43 + tt) * 128 : OOB, 0, 0);
    SB();
    asm volatile("" : "+v"(bbase));
    const u32 tag = get_tag(ep);
    {
      const int o0 = rw * 128 + (((c8 >> 1) ^ oct_sw(rw)) * 32) + (c8 & 1) * 16;
      *(LAS v4u*)(lds + L_OPX + o0) = x0;
      *(LAS v4u*)(lds + L_OPX + o0 + 4096) = x1;
      lds_arrive(sy + SY_XI, l);
      float s1 = 0.f, s2 = 0.f;
#pragma unroll
      for (int i = 0; i < 4; ++i) {
        s1 = dot2(xa[i], 0x3f803f80u, s1);
        s2 = dot2(xb[i], 0x3f803f80u, s2);
      }
      const float m1 = wave_sum(s1) * (1.0f / D_), m2 = wave_sum(s2) * (1.0f / D_);
      float q1 = 0.f, q2 = 0.f;
#pragma unroll
      for (int i = 0; i < 4; ++i) { q1 = sqdev(xa[i], m1, q1); q2 = sqdev(xb[i], m2, q2); }
      q1 = wave_sum(q1) * (1.0f / D_);
      q2 = wave_sum(q2) * (1.0f / D_);
      const u32 sA = ln_stats(l & 1 ? m2 : m1, l & 1 ? q2 : q1);
      if (l < 2) st8(rs, l * 8, OFF_ST1 + (b >> 6) * 4096 + wq * 1024 + (b & 63) * 16, v2u{sA, tag});
      lds_wait(sy + SY_XI, 4);
      const int rr = 8 * g + q;
      LAS char* xp = lds + L_OPX + rr * 128 + ((wq ^ oct_sw(rr)) * 32) + p * 8;
#pragma unroll
      for (int u = 0; u < 2; ++u) {
        const v2u ya = tr_read(xp + u * 4096), yb = tr_read(xp + u * 4096 + 512);
        st16(rs, (l & 15) * 4096 + g * 16 + u * 64, OFF_XT + (64 * grp + 16 * wq) * 4096 + j * 128, v4u{ya[0], ya[1], yb[0], yb[1]});
      }
    }
    STAMP(13);
    LAS char* gsc = lds + L_GSC + wq * 4096;
    if (do_mod) {
      v2f acc[4];
#pragma unroll
      for (int e = 0; e < 4; ++e) acc[e] = v2f{0.f, 0.f};
#pragma unroll
      for (int i = 0; i < 4; ++i) {
        const float c = bf_lo(cc[i]);
        const float s = rbf(c * __builtin_amdgcn_rcpf(1.0f + __expf(-c)));
#pragma unroll
        for (int e = 0; e < 4; ++e) acc[e] = fma2(up2(tv[0][i][e]), v2f{s, s}, acc[e]);
      }
      float r[8];
#pragma unroll
      for (int e = 0; e < 8; ++e) {
        float a = acc[e >> 1][e & 1];
        a += dpp_mov<0x124>(a);
        a += dpp_mov<0x128>(a);
        r[e] = a;
      }
      if ((l & 12) == 0) {
        LAS char* mp = lds + L_MRED + (4 * wq + g) * 128 + p * 32;
        *(LAS v4f*)mp = v4f{r[0], r[1], r[2], r[3]};
        *(LAS v4f*)(mp + 16) = v4f{r[4], r[5], r[6], r[7]};
      }
      lds_arrive(sy + SY_MOD, l);
      if (wq == 0) {
        lds_wait(sy + SY_MOD, 4);
        LAS float* mr = (LAS float*)(lds + L_MRED) + l;
        float s = 0.f;
#pragma unroll
        for (int i = 0; i < 8; ++i) s += mr[64 * i];
        s = xor32_add(s);
        if (l < 32) st8(rs, l * 16, OFF_MOD + mcg * 512 + mks * 8, v2u{as_u(s), tag});
      }
    } else {
#pragma unroll
      for (int n = 0; n < 2; ++n) {
        if (n == 0 || b < 24) {
          const int tc = (b >> 2) + 16 * n;
#pragma unroll
          for (int i = 0; i < 4; ++i) *(LAS v4u*)(gsc + i * 1024 + l * 16) = tv[n][i];
          LAS char* rp = gsc + (4 * g + q) * 128 + 8 * p;
          v4u o[4];
#pragma unroll
          for (int a = 0; a < 4; ++a) {
            const v2u h0 = tr_read(rp + 32 * a), h1 = tr_read(rp + 32 * a + 2048);
            o[a] = v4u{h0[0], h0[1], h1[0], h1[1]};
          }
#pragma unroll
          for (int a = 0; a < 4; ++a) st16(rs, 4 * tc + a < 86 ? l * 16 : OOB, OFF_W1T + ((4 * tc + a) * 16 + tk) * 1024, o[a]);
        }
      }
    }
    STAMP(1);
    publish<4>(sy + SY_XCNT, flag_ptr(ws, FL_X + grp * 32 + j), tag, l);
    asm volatile("" :: "v"(wpf));
    STAMP(2);
    if (wq == 3) {
      const int tcq = (l >> 2) & 3;
      const int pbk = 4 * (tcq < 2 ? 2 * cg + tcq : (sg0 + (tcq & 1) * (nsg - 1)) >> 2) + (l & 3);
      poll_flags<2>(flag_ptr(ws, FL_X + (l < 32 ? grp * 32 + l : (pbk & 7) * 32 + (pbk >> 3))), l < 48, tag);
      lds_arrive(sy + SY_XGO, l);
    } else {
      lds_wait(sy + SY_XGO, 1);
    }
    STAMP(12);
    const int kp = 4 * cg + wq;
    const int sgw = sg0 + (wq < nsg ? wq : 0);
    const int voff = l * 16;
    v4u A[16][2];
    auto w1_load = [&](int ks) {
      A[ks][0] = ld16(rs, voff + (ks & 3) * 1024, OFF_W1T + 2 * kp * 16384 + (ks >> 2) * 4096);
      A[ks][1] = ld16(rs, voff + (ks & 3) * 1024, OFF_W1T + (2 * kp + 1) * 16384 + (ks >> 2) * 4096);
    };
    {
      const int dc = 64 * grp + 2 * j;
      const v4u xc0 = ld16(rs, tt * 16, OFF_XT + dc * 4096), xc1 = ld16(rs, tt * 16, OFF_XT + (dc + 1) * 4096);
      const u32 b2v = b2[64 * cg + l];
      v4u stq[4], mown, mt[4], mg;
      auto urgent = [&]() {
#pragma unroll
        for (int i = 0; i < 4; ++i) stq[i] = ld16(rs, voff + i * 1024, OFF_ST1 + wq * 4096);
        const int wh_ = (l >> 1) & 3;
        mown = ld16(rs, (wh_ == 3 ? 4 : wh_) * 8192 + (l & 1) * 16, OFF_MOD + dc * 16);
      };
      auto tables = [&]() {
#pragma unroll
        for (int i = 0; i < 4; ++i) mt[i] = ld16(rs, voff + i * 1024, OFF_MOD + 1536 * 16 + wq * 4096);
        mg = ld16(rs, voff, OFF_MOD + (2560 + 64 * cg) * 16);
      };
      urgent();
      tables();
#pragma unroll
      for (int ks = 0; ks < W1A; ++ks) w1_load(ks);
      SB();
#pragma nounroll
      for (int spin = 0; spin < SPIN_CAP; ++spin) {
        const u32 bad = tagdiff(mown, tag) | tagdiff(stq[0], tag) | tagdiff(stq[1], tag) | tagdiff(stq[2], tag) | tagdiff(stq[3], tag);
        if (!__any(bad != 0u)) break;
        __builtin_amdgcn_s_sleep(4);
        asm volatile("" ::: "memory");
        urgent();
      }
      const int mo = __builtin_bit_cast(int, sum2r(mown));
      if (tt == 6 || tt == 7) sy[SY_C1 + (tt & 1)] = as_u(rbf(1.0f + as_f(mo)));
      *(LAS v4u*)(lds + L_OPX + tt * 16) = xc0;
      *(LAS v4u*)(lds + L_OPX + 4096 + tt * 16) = xc1;
      v2f mu[4], ri[4];
#pragma unroll
      for (int i = 0; i < 4; ++i) {
        mu[i] = v2f{bf_lo(stq[i][0]), bf_lo(stq[i][2])};
        ri[i] = v2f{__builtin_amdgcn_rcpf(bf_hi(stq[i][0])), __builtin_amdgcn_rcpf(bf_hi(stq[i][2]))};
      }
#pragma unroll
      for (int chn = 0; chn < 2; ++chn) {
        const v4u xc = chn ? xc1 : xc0;
        const float sa = as_f(__builtin_amdgcn_readlane(mo, chn));
        const float ca1 = rbf(1.0f + as_f(__builtin_amdgcn_readlane(mo, 2 + chn)));
        const float ga = as_f(__builtin_amdgcn_readlane(mo, 4 + chn));
        const float gc = ga * ca1, gs = ga * sa;
        v2f hg[4];
#pragma unroll
        for (int i = 0; i < 4; ++i) hg[i] = fma2((up2(xc[i]) - mu[i]) * ri[i], v2f{gc, gc}, v2f{gs, gs});
        *(LAS v4f*)(lds + L_OPH + chn * 8192 + tt * 32) = v4f{hg[0][0], hg[0][1], hg[1][0], hg[1][1]};
        *(LAS v4f*)(lds + L_OPH + chn * 8192 + tt * 32 + 16) = v4f{hg[2][0], hg[2][1], hg[3][0], hg[3][1]};
      }
      lds_arrive(sy + SY_OPS, l);
      STAMP(4);
#pragma nounroll
      for (int spin = 0; spin < SPIN_CAP; ++spin) {
        const u32 bad = tagdiff(mg, tag) | tagdiff(mt[0], tag) | tagdiff(mt[1], tag) | tagdiff(mt[2], tag) | tagdiff(mt[3], tag);
        if (!__any(bad != 0u)) break;
        __builtin_amdgcn_s_sleep(4);
        asm volatile("" ::: "memory");
        tables();
      }
      const float one = wq >= 2 ? 1.0f : 0.0f;
#pragma unroll
      for (int i = 0; i < 4; ++i) *(LAS u16*)(lds + L_TB + (256 * wq + 64 * i + l) * 2) = (u16)pack2(one + sum2r(mt[i]), 0.f);
      if (wq == 0) {
        *(LAS float*)(lds + L_GB + l * 4) = sum2r(mg);
        *(LAS float*)(lds + L_GB + 256 + l * 4) = bf_lo(b2v);
      }
      lds_arrive(sy + SY_TAB, l);
    }
    SB();
    {
      v4u Cc[2][4];
      const int voff3 = wq < nsg ? voff : OOB;
      auto chunk = [&](int kq) {
#pragma unroll
        for (int i = 0; i < 4; ++i) {
          Cc[kq & 1][i] = ld16(rs, voff3 + i * 1024, OFF_W1T + (64 + sgw) * 16384 + kq * 4096);
          if (4 * kq + i >= W1A) w1_load(4 * kq + i);
        }
      };
      const u32 b1v = b1[l < 32 ? 32 * kp + l : 1024 + 16 * sgw + (l & 15)];
      chunk(0);
      chunk(1);
      SB();
      STAMP(3);
      lds_wait(sy + SY_TAB, 4);
      lds_wait(sy + SY_ARD, 4);
      v4f tacc[3];
#pragma unroll
      for (int a = 0; a < 3; ++a) tacc[a] = v4f{0.f, 0.f, 0.f, 0.f};
      LAS char* tbp = lds + L_TB + (((l >> 3) & 1) ^ 1) * 1024 + 8 * g;
      LAS char* w1c = wq < 3 ? lds + L_W1C + wq * 16384 + l * 16 : gsc + l * 16;
      const int wst = wq < 3 ? 1024 : 0;
#pragma unroll
      for (int kq = 0; kq < 4; ++kq) {
        v2u tb[4][2];
#pragma unroll
        for (int i = 0; i < 4; ++i) {
          tb[i][0] = *(LAS v2u*)(tbp + 64 * (4 * kq + i));
          tb[i][1] = *(LAS v2u*)(tbp + 64 * (4 * kq + i) + 32);
        }
        SB();
#pragma unroll
        for (int i = 0; i < 4; ++i) {
          const int ks = 4 * kq + i;
          const v4u t4 = {tb[i][0][0], tb[i][0][1], tb[i][1][0], tb[i][1][1]};
          *(LAS v4u*)(w1c + ks * wst) = Cc[kq & 1][i];
          tacc[2] = mfma16(Cc[kq & 1][i], t4, tacc[2]);
          tacc[0] = mfma16(A[ks][0], t4, tacc[0]);
          tacc[1] = mfma16(A[ks][1], t4, tacc[1]);
        }
        SB();
        if (kq < 2) chunk(kq + 2);
        SB();
      }
      if ((l & 7) == 0) {
#pragma unroll
        for (int a = 0; a < 3; ++a) *(LAS v4f*)(lds + L_TT + (a < 2 ? 2 * wq + a : 8 + wq) * 128 + (l & 8) * 8 + 16 * g) = tacc[a];
      }
      if (l < 48) *(LAS float*)(lds + L_TT + (l < 32 ? 2 * wq + (l >> 4) : 8 + wq) * 128 + 64 + (l & 15) * 4) += bf_lo(b1v);
    }
    STAMP(5);
    if (wq == 3) {
      lds_wait(sy + SY_BCNT, 4);
      poll_flags<2>(flag_ptr(ws, FL_B + grp * 8 + (l & 7)), l < 8, bbase + 32u);
      lds_arrive(sy + SY_BGO, l);
    } else {
      lds_wait(sy + SY_BGO, 1);
    }
    {
      const v4u xr0 = ld16(rs, voff, OFF_X1B + rt * 65536 + cg * 8192 + wq * 2048);
      const v4u xr1 = ld16(rs, voff + 1024, OFF_X1B + rt * 65536 + cg * 8192 + wq * 2048);
      stage_u(lds, rs, rt, w, l);
      const int r8 = 16 * wq + (l >> 3);
      *(LAS v4u*)(lds + L_XS + r8 * 128 + ((((l & 7) >> 1) ^ oct_sw(r8)) * 32) + (l & 1) * 16) = xr0;
      *(LAS v4u*)(lds + L_XS + (r8 + 8) * 128 + ((((l & 7) >> 1) ^ oct_sw(r8 + 8)) * 32) + (l & 1) * 16) = xr1;
    }
    WG_BARRIER();
    STAMP(6);
    v4f acc1[2][4];
    const int pb = OFF_PST + (rt * 4 + wq) * 2048;
    v4u pa, pc, W[4][4];
    {
      const int wo = (l >> 3) * 1024 + (l & 7) * 16;
      const int sw = (2 * g + (q >> 1)) & 3;
      LAS char* rb[4];
#pragma unroll
      for (int i = 0; i < 4; ++i) rb[i] = lds + (4 * g + q) * 128 + ((i ^ sw) * 32) + p * 8;
#pragma unroll
      for (int a = 0; a < 2; ++a)
#pragma unroll
        for (int i = 0; i < 4; ++i) acc1[a][i] = v4f{0.f, 0.f, 0.f, 0.f};
      v4u B[2][4];
      auto fetch = [&](int ks) {
#pragma unroll
        for (int i = 0; i < 4; ++i) {
          v2u h0 = tr_read(rb[i] + ks * 4096), h1 = tr_read(rb[i] + ks * 4096 + 2048);
          B[ks & 1][i] = v4u{h0[0], h0[1], h1[0], h1[1]};
        }
      };
      fetch(0);
#pragma unroll
      for (int ks = 0; ks < 16; ++ks) {
        if (ks >= 2 && ks < 10) {
          const int n = (ks - 2) >> 1, kap = n == 0 ? 28 + wq : 31 + 3 * wq + n;
#pragma unroll
          for (int i = 2 * (ks & 1); i < 2 * (ks & 1) + 2; ++i)
            W[n][i] = __builtin_amdgcn_raw_buffer_load_b128(rw2, kap < NK2 ? wo : OOB, (32 * kap + 8 * i) * 1024 + cg * 128, 0);
        }
        if (ks == 10) { pa = ld16(rs, voff, pb); pc = ld16(rs, voff + 1024, pb); }
        SB();
        if (ks < 15) fetch(ks + 1);
#pragma unroll
        for (int i = 0; i < 4; ++i) {
          acc1[0][i] = mfma16(A[ks][0], B[ks & 1][i], acc1[0][i]);
          acc1[1][i] = mfma16(A[ks][1], B[ks & 1][i], acc1[1][i]);
        }
#pragma unroll
        for (int i = 0; i < 8; ++i) {
          __builtin_amdgcn_sched_group_barrier(0x8, 1, 0);
          __builtin_amdgcn_sched_group_barrier(0x100, 1, 0);
        }
      }
    }
    SB();
    STAMP(7);
    {
      if (__any((tagdiff(pa, tag) | tagdiff(pc, tag)) != 0u)) wait_partials(rs, pb, l, tag, pa, pc);
      const u32 sp = combine_stats(pa, pc);
      const float rstd = __builtin_amdgcn_rcpf(bf_hi(sp));
      if (l < 16) *(LAS v2f*)(lds + L_ST2 + (wq * 16 + l) * 8) = v2f{rstd, -bf_lo(sp) * rstd};
      lds_arrive(sy + SY_ST2, l);
    }
    v4f tq[2][2];
#pragma unroll
    for (int a = 0; a < 2; ++a) {
      tq[a][0] = *(LAS v4f*)(lds + L_TT + (2 * wq + a) * 128 + 16 * g);
      tq[a][1] = *(LAS v4f*)(lds + L_TT + (2 * wq + a) * 128 + 64 + 16 * g);
    }
    lds_wait(sy + SY_ST2, 4);
    STAMP(14);
#pragma unroll
    for (int i = 0; i < 4; ++i) {
      const v2f stt = *(LAS v2f*)(lds + L_ST2 + (i * 16 + (l & 15)) * 8);
      const v2u o0 = act4(acc1[0][i], stt[0], stt[1], tq[0][0], tq[0][1]), o1 = act4(acc1[1][i], stt[0], stt[1], tq[1][0], tq[1][1]);
      st16(rs, voff, OFF_HID + ((4 * rt + i) * NK2 + kp) * 1024, v4u{o0[0], o0[1], o1[0], o1[1]});
    }
    STAMP(15);
    publish<8>(sy + SY_CCNT, flag_ptr(ws, FL_C + rt * 8 + cg), tag, l);
    STAMP(8);
#pragma unroll
    for (int i = 0; i < 4; ++i) w2_transpose(gsc, W[i], l);
    SB();
    lds_wait(sy + SY_CGO, 4);
    STAMP(9);
    const int oob = wq == 3 ? OOB : 0;
    v4u H[4][4];
#pragma unroll
    for (int s = 0; s < 4; ++s) H[0][s] = ld16(rs, voff, OFF_HID + ((4 * rt + s) * NK2 + 28 + wq) * 1024);
#pragma unroll
    for (int i = 0; i < 3; ++i)
#pragma unroll
      for (int s = 0; s < 4; ++s) {
        const int so = OFF_HID + ((4 * rt + s) * NK2 + 32 + 3 * wq + i) * 1024;
        const int vo = i == 2 ? l * 8 + oob : l * 8;
        v2u h0 = ld8(rs, vo, so), h1 = ld8(rs, vo + 512, so);
        H[1 + i][s] = v4u{h0[0], h0[1], h1[0], h1[1]};
      }
    SB();
    v4f acc[4][4];
#pragma unroll
    for (int a = 0; a < 4; ++a)
#pragma unroll
      for (int s = 0; s < 4; ++s) acc[a][s] = v4f{0.f, 0.f, 0.f, 0.f};
#pragma unroll
    for (int i = 0; i < 4; ++i)
#pragma unroll
      for (int s = 0; s < 4; ++s)
#pragma unroll
        for (int a = 0; a < 4; ++a) acc[a][s] = mfma16(W[i][a], H[i][s], acc[a][s]);
    dump_acc(lds + w * 16384, acc, l);
    STAMP(10);
    WG_BARRIER();
    final_epilogue(lds, out, rt, cg, w, l);
    STAMP(11);
    STAMP_FLUSH();
  }
}

extern "C" void kernel_launch(void* const* d_in, const int* in_sizes, int n_in, void* d_out, int out_size,
                              void* d_ws, size_t ws_size, hipStream_t stream) {
  fused_block<<<256, 512, 0, stream>>>((const u16*)d_in[0], (const u16*)d_in[1], (const u16*)d_in[2], (const u16*)d_in[3],
                                       (const u16*)d_in[4], (const u16*)d_in[5], (const u16*)d_in[7], (const u16*)d_in[8],
                                       (const u16*)d_in[9], (const u16*)d_in[10], (const u16*)d_in[11], (const u16*)d_in[12],
                                       (char*)d_ws, (u16*)d_out);
}
```

```cpp
#include <hip/hip_runtime.h>
#include <hip/hip_bf16.h>
#include <stdint.h>

typedef unsigned short u16;
typedef unsigned int u32;
typedef u32 v4u __attribute__((ext_vector_type(4)));
typedef u32 v2u __attribute__((ext_vector_type(2)));
typedef float v4f __attribute__((ext_vector_type(4)));
typedef float v2f __attribute__((ext_vector_type(2)));
typedef short v4s __attribute__((ext_vector_type(4)));
typedef __bf16 v8bf __attribute__((ext_vector_type(8)));
typedef __bf16 v2bf __attribute__((ext_vector_type(2)));
#define LAS __attribute__((address_space(3)))
#define GAS __attribute__((address_space(1)))
#define CAS __attribute__((address_space(4)))
#define DI __device__ __forceinline__
#define SB() __builtin_amdgcn_sched_barrier(0)
#define DRAIN() asm volatile("s_waitcnt vmcnt(0)" ::: "memory")
#define LGKM0() asm volatile("s_waitcnt lgkmcnt(0)" ::: "memory")
#define WG_BARRIER() do { SB(); LGKM0(); __builtin_amdgcn_s_barrier(); asm volatile("" ::: "memory"); SB(); } while (0)

constexpr int S_ = 2048, D_ = 512, F_ = 1376;
constexpr int NK2 = F_ / 32;

constexpr int OFF_KVT = 0;
constexpr int OFF_XT  = OFF_KVT + D_ * S_ * 2;
constexpr int OFF_X1B = OFF_XT + D_ * S_ * 2;
constexpr int OFF_UB  = OFF_X1B + D_ * S_ * 2;
constexpr int OFF_HID = OFF_UB + D_ * S_ * 2;
constexpr int OFF_W1T = OFF_HID + S_ * F_ * 2;
constexpr int OFF_W2T = OFF_W1T + D_ * F_ * 2;
constexpr int OFF_ST1 = OFF_W2T + D_ * F_ * 2;
constexpr int OFF_MOD = OFF_ST1 + S_ * 8;
constexpr int OFF_PST = OFF_MOD + 4 * 3072 * 8;
constexpr int OFF_FLG = OFF_PST + 32 * 4 * 8 * 256;
constexpr int FL_A = 0, FL_C = 256, FL_D = 512, FL_B = 768, FL_T = 832, FL_U = FL_T + 112, FL_EPOCH = FL_U + 176;
constexpr int WS_END  = OFF_FLG + (FL_EPOCH + 1) * 128;
constexpr int OOB = 0x7fff0000;

constexpr int L_U = 0, U_BYTES = 4096 + 512;
constexpr int L_XCH = 4 * U_BYTES;
constexpr int L_KIMG = L_XCH, L_VIMG = L_XCH + 68 * 128;
constexpr int L_OPX = L_XCH + 17408;
constexpr int L_OPH = L_OPX + 8192;
constexpr int L_MRED = L_OPH + 16384;
constexpr int L_W1C = 65536;
constexpr int CP_STRIDE = 4160, CH_FILT = 8 * CP_STRIDE;
constexpr int L_FILT = 65536;
constexpr int L_TB = L_FILT + 2 * CH_FILT;
constexpr int L_TT = L_TB + 2048;
constexpr int L_ST2 = L_TT + 1536;
constexpr int L_GB = L_ST2 + 512;
constexpr int L_XS = L_GB + 512;
constexpr int L_GSC = L_XS + 8192;
constexpr int L_SYNC = L_GSC + 16384;
constexpr int LDS_BYTES = L_SYNC + 256;
static_assert(L_MRED + 4096 <= 65536 && LDS_BYTES <= 163840, "lds");
enum { SY_KV, SY_A1CNT, SY_A1GO, SY_PAIR0, SY_PAIR1, SY_TAB, SY_BCNT, SY_MOD, SY_ST2, SY_ST2B, SY_CCNT, SY_DCNT, SY_FB0, SY_FB1, SY_ARD, SY_XS,
       SY_BGO, SY_WOK, SY_XCNT, SY_TCNT, SY_STO, SY_ROW, SY_CGO, SY_DMP = SY_CGO + 4, SY_C1GO = SY_DMP + 4, SY_END = SY_C1GO + 4 };

constexpr int W2B = 16;
constexpr int SGC = 4;
constexpr int PSTK = 5;
constexpr int PUBK = 8;

DI int lane_id() {
  int l = __builtin_amdgcn_mbcnt_hi(~0u, __builtin_amdgcn_mbcnt_lo(~0u, 0u));
  asm volatile("" : "+v"(l));
  __builtin_assume(l >= 0 && l < 64);
  return l;
}
DI float bf_lo(u32 w) { return __builtin_bit_cast(float, w << 16); }
DI float bf_hi(u32 w) { return __builtin_bit_cast(float, w & 0xffff0000u); }
DI float bf1(u16 h) { return __builtin_bit_cast(float, (u32)h << 16); }
DI u32 pack2(float a, float b) {
  v2f f = {a, b};
  return __builtin_bit_cast(u32, __builtin_convertvector(f, v2bf));
}
DI float rbf(float a) { return bf_lo(pack2(a, a)); }
DI v4f mfma16(v4u a, v4u b, v4f c) {
  return __builtin_amdgcn_mfma_f32_16x16x32_bf16(__builtin_bit_cast(v8bf, a), __builtin_bit_cast(v8bf, b), c, 0, 0, 0);
}
template <int CTRL> DI float dpp_mov(float v) {
  return __builtin_bit_cast(float, __builtin_amdgcn_update_dpp(0, __builtin_bit_cast(int, v), CTRL, 0xf, 0xf, true));
}
DI float xor16_add(float v) {
  u32 u = __builtin_bit_cast(u32, v);
  auto r = __builtin_amdgcn_permlane16_swap(u, u, false, false);
  return __builtin_bit_cast(float, (u32)r[0]) + __builtin_bit_cast(float, (u32)r[1]);
}
DI float xor32_add(float v) {
  u32 u = __builtin_bit_cast(u32, v);
  auto r = __builtin_amdgcn_permlane32_swap(u, u, false, false);
  return __builtin_bit_cast(float, (u32)r[0]) + __builtin_bit_cast(float, (u32)r[1]);
}
DI float wave_sum(float v) {
  v += dpp_mov<0xB1>(v);
  v += dpp_mov<0x4E>(v);
  v += dpp_mov<0x141>(v);
  v += dpp_mov<0x140>(v);
  v = xor16_add(v);
  v = xor32_add(v);
  return v;
}
DI float dot2(u32 a, u32 b, float c) {
  return __builtin_amdgcn_fdot2_f32_bf16(__builtin_bit_cast(v2bf, a), __builtin_bit_cast(v2bf, b), c, false);
}
DI u32 ln_stats(float mean, float var) { return pack2(mean, __builtin_amdgcn_sqrtf(rbf(rbf(var) + 1e-5f))); }
DI v2u tr_read(LAS char* p) { return __builtin_bit_cast(v2u, __builtin_amdgcn_ds_read_tr16_b64_v4i16((LAS v4s*)p)); }
DI v4u ldin(const void* p) { return *(const v4u*)p; }
DI float as_f(u32 u) { return __builtin_bit_cast(float, u); }
DI u32 as_u(float f) { return __builtin_bit_cast(u32, f); }
DI u32 tagdiff(v4u p, u32 tag) { return (p[1] ^ tag) | (p[3] ^ tag); }

typedef __amdgpu_buffer_rsrc_t rsrc_t;
constexpr int SC1 = 16;
DI v4u ld16(rsrc_t r, int off, int soff) { return __builtin_amdgcn_raw_buffer_load_b128(r, off, soff, SC1); }
DI v2u ld8(rsrc_t r, int off, int soff) { return __builtin_amdgcn_raw_buffer_load_b64(r, off, soff, SC1); }
DI void st16(rsrc_t r, int off, int soff, v4u v) { __builtin_amdgcn_raw_buffer_store_b128(v, r, (int)((u32)off + (u32)soff), 0, SC1); }
DI void st8(rsrc_t r, int off, int soff, v2u v) { __builtin_amdgcn_raw_buffer_store_b64(v, r, off, soff, SC1); }

constexpr int SPIN_CAP = 1 << 18;
DI GAS u32* flag_ptr(char* ws, int idx) { return (GAS u32*)(ws + OFF_FLG + idx * 128); }
template <int SLEEP> DI void poll_flags(GAS u32* f, bool act, u32 tag) {
  bool pend = act;
#pragma nounroll
  for (int spin = 0; spin < SPIN_CAP; ++spin) {
    if (pend) pend = __hip_atomic_load(f, __ATOMIC_RELAXED, __HIP_MEMORY_SCOPE_AGENT) != tag;
    if (!__any(pend)) break;
    __builtin_amdgcn_s_sleep(SLEEP);
  }
  asm volatile("" ::: "memory");
}
DI void lds_arrive(LAS u32* c, int l) {
  LGKM0();
  if (l == 0) __hip_atomic_fetch_add(c, 1u, __ATOMIC_RELAXED, __HIP_MEMORY_SCOPE_WORKGROUP);
}
DI void lds_signal(LAS u32* c, int l) {
  asm volatile("" ::: "memory");
  if (l == 0) __hip_atomic_fetch_add(c, 1u, __ATOMIC_RELAXED, __HIP_MEMORY_SCOPE_WORKGROUP);
}
DI void lds_wait(LAS u32* c, u32 n) {
#pragma nounroll
  for (int spin = 0; spin < (SPIN_CAP << 4); ++spin) {
    if (__hip_atomic_load(c, __ATOMIC_RELAXED, __HIP_MEMORY_SCOPE_WORKGROUP) >= n) break;
    __builtin_amdgcn_s_sleep(1);
  }
  asm volatile("" ::: "memory");
}
template <int N> DI void publish(LAS u32* c, GAS u32* f, u32 tag, int l) {
  DRAIN();
  if (l == 0) {
    u32 old = __hip_atomic_fetch_add(c, 1u, __ATOMIC_RELAXED, __HIP_MEMORY_SCOPE_WORKGROUP);
    if (old == N - 1) __hip_atomic_store(f, tag, __ATOMIC_RELAXED, __HIP_MEMORY_SCOPE_AGENT);
  }
}
template <int N> DI void publish_count(LAS u32* c, char* ws, int first, int shard, int l) {
  DRAIN();
  u32 old = 0;
  if (l == 0) old = __hip_atomic_fetch_add(c, 1u, __ATOMIC_RELAXED, __HIP_MEMORY_SCOPE_WORKGROUP);
  if (__builtin_amdgcn_readfirstlane(old) == N - 1 && l < 8)
    __hip_atomic_fetch_add(flag_ptr(ws, first + l * 8 + shard), 1u, __ATOMIC_RELAXED, __HIP_MEMORY_SCOPE_AGENT);
}
template <int V> struct IC { static constexpr int v = V; };
DI u32 get_tag(u32 ep) {
  u32 tag = (u32)__builtin_amdgcn_readfirstlane(ep) * 0x9E3779B1u + 0x7F4A7C15u;
  asm volatile("" : "+s"(tag));
  return tag;
}
DI int oct_sw(int row) { return ((row >> 1) & 1) | ((row >> 2) & 2); }

DI v2f combine_stats(v4u pa, v4u pc) {
  const float m = xor32_add(xor16_add(as_f(pa[0]) + as_f(pc[0]))) * (1.0f / D_);
  const float da = as_f(pa[0]) * (1.0f / 64) - m, dc = as_f(pc[0]) * (1.0f / 64) - m;
  const float m2 = xor32_add(xor16_add((as_f(pa[2]) + as_f(pc[2])) + 64.f * (da * da + dc * dc)));
  const float rstd = __builtin_amdgcn_rsqf(m2 * (1.0f / D_) + 1e-5f);
  return v2f{rstd, -m * rstd};
}
DI void wait_partials(rsrc_t rs, int pb, int l, u32 tag, v4u& pa, v4u& pc) {
#pragma nounroll
  for (int spin = 0; spin < SPIN_CAP; ++spin) {
    asm volatile("" ::: "memory");
    pa = ld16(rs, l * 16, pb);
    pc = ld16(rs, l * 16 + 1024, pb);
    if (!__any((tagdiff(pa, tag) | tagdiff(pc, tag)) != 0u)) break;
    __builtin_amdgcn_s_sleep(2);
  }
}
DI float sqdev(u32 x, float m, float acc) {
  const float e0 = bf_lo(x) - m, e1 = bf_hi(x) - m;
  return acc + e0 * e0 + e1 * e1;
}
DI v2f lo2(v4f v) { return __builtin_shufflevector(v, v, 0, 1); }
DI v2f hi2(v4f v) { return __builtin_shufflevector(v, v, 2, 3); }
DI v2f fma2(v2f a, v2f b, v2f c) { return __builtin_elementwise_fma(a, b, c); }
DI v2f up2(u32 w) { return v2f{bf_lo(w), bf_hi(w)}; }
DI u32 pk2(v2f v) { return __builtin_bit_cast(u32, __builtin_convertvector(v, v2bf)); }
DI v2f dup2(float v) {
  v2f r = {v, v};
  asm volatile("" : "+v"(r));
  return r;
}
DI v2u act4(v4f acc, v2f r2, v2f m2, v4f t1, v4f t2) {
  v2f p0 = fma2(lo2(acc), r2, fma2(lo2(t1), m2, lo2(t2))), p1 = fma2(hi2(acc), r2, fma2(hi2(t1), m2, hi2(t2)));
  p0 = v2f{fmaxf(p0[0], 0.f), fmaxf(p0[1], 0.f)};
  p1 = v2f{fmaxf(p1[0], 0.f), fmaxf(p1[1], 0.f)};
  return v2u{pk2(p0 * p0), pk2(p1 * p1)};
}
DI v4u tb_frag(LAS char* tbp, int ks) {
  return *(LAS v4u*)(tbp + 64 * ks);
}
template <int N> DI void stage_load(v4u (&xs)[N], rsrc_t rs, int rt, int k0, int l) {
#pragma unroll
  for (int i = 0; i < N; ++i) xs[i] = ld16(rs, l * 16 + (i & 3) * 1024, OFF_UB + rt * 65536 + k0 * 1024 + (i >> 2) * 4096);
}
template <int N> DI void stage_write(const v4u (&xs)[N], LAS char* lds, int k0, int l) {
  const int c16 = l & 7, r8 = l >> 3;
  LAS char* dst = lds + k0 * 1024 + r8 * 128 + (((c16 >> 1) ^ ((r8 >> 1) & 3)) * 32) + (c16 & 1) * 16;
#pragma unroll
  for (int i = 0; i < N; ++i) *(LAS v4u*)(dst + i * 1024) = xs[i];
}
DI void final_epilogue(LAS char* lds, u16* out, int rt, int db, int st, int hf, int l) {
  const rsrc_t ro = __builtin_amdgcn_make_buffer_rsrc((void*)out, 0, S_ * D_ * 2, 0x00020000);
  const int g = l >> 4, q = (l & 15) >> 2, p = l & 3;
  u32 o[4];
#pragma unroll
  for (int a = 0; a < 2; ++a) {
    v4f v[8];
#pragma unroll
    for (int k = 0; k < 8; ++k) v[k] = *(LAS v4f*)(lds + k * 16384 + ((st * 4 + 2 * hf + a) * 64 + l) * 16);
    const int dl = 32 * hf + 8 * g + 4 * a + q;
    const v2u xq = tr_read(lds + L_XS + dl * 128 + ((st ^ oct_sw(dl)) * 32) + p * 8);
    const v4f gb = *(LAS v4f*)(lds + L_GB + (32 * hf + 8 * g + 4 * a) * 4);
    const v4f bb = *(LAS v4f*)(lds + L_GB + 256 + (32 * hf + 8 * g + 4 * a) * 4);
    const v4f s = ((v[0] + v[1]) + (v[2] + v[3])) + ((v[4] + v[5]) + (v[6] + v[7]));
    u32 p0 = pk2(lo2(s)), p1 = pk2(hi2(s));
    p0 = pk2(up2(p0) + lo2(bb));
    p1 = pk2(up2(p1) + hi2(bb));
    p0 = pk2(up2(p0) * lo2(gb));
    p1 = pk2(up2(p1) * hi2(gb));
    o[2 * a] = pk2(up2(xq[0]) + up2(p0));
    o[2 * a + 1] = pk2(up2(xq[1]) + up2(p1));
  }
  st16(ro, ((l & 15) * D_ + 8 * g) * 2, ((64 * rt + 16 * st) * D_ + 64 * db + 32 * hf) * 2, v4u{o[0], o[1], o[2], o[3]});
}

__global__ __launch_bounds__(512, 2) void fused_block(const u16* __restrict__ x, const u16* __restrict__ k, const u16* __restrict__ v,
                                                      const u16* __restrict__ shk, const u16* __restrict__ ssmk,
                                                      const u16* __restrict__ ssmD, const u16* __restrict__ w1,
                                                      const u16* __restrict__ b1, const u16* __restrict__ w2,
                                                      const u16* __restrict__ b2, const u16* __restrict__ cvec,
                                                      const u16* __restrict__ wmod, char* ws, u16* __restrict__ out) {
  __shared__ __attribute__((aligned(16))) char lds_raw[LDS_BYTES];
  LAS char* lds = (LAS char*)lds_raw;
  LAS u32* sy = (LAS u32*)(lds + L_SYNC);
  const int b = blockIdx.x;
  const int w = __builtin_amdgcn_readfirstlane(threadIdx.x >> 6), wq = w & 3;
  const int grp = b & 7, j = b >> 3;
  const int rt = 8 * (grp >> 1) + (j >> 2), cg = 4 * (grp & 1) + (j & 3);
  const int nsg = cg < 6 ? 3 : 2, sg0 = cg < 6 ? 3 * cg : 2 * cg + 6;
  const rsrc_t rs = __builtin_amdgcn_make_buffer_rsrc(ws, 0, WS_END, 0x00020000);
  asm volatile("" :: "s"(x), "s"(k), "s"(v), "s"(shk), "s"(ssmk), "s"(ssmD), "s"(w1), "s"(b1), "s"(w2), "s"(b2), "s"(cvec),
               "s"(wmod), "s"(ws), "s"(out));

  if (__builtin_expect(w < 4, 1)) {
    const int l = lane_id(), t = 64 * w + l;
    __builtin_amdgcn_s_setprio(2);
    const int ch = wq >> 1, wh = wq & 1;
    const int d = 64 * grp + 2 * j + ch;
    const int c = l & 15, g = l >> 4, q = c >> 2, p = l & 3;
    const int rw = t >> 3, c8 = t & 7;
    const u32 toff = (u32)(64 * j + rw) * (D_ * 2) + (u32)(64 * grp + 8 * c8) * 2;
    const v4u k0 = ldin((const char*)k + toff), k1 = ldin((const char*)k + toff + 32 * D_ * 2);
    const v4u v0 = ldin((const char*)v + toff), v1 = ldin((const char*)v + toff + 32 * D_ * 2);
    v4u kh = ldin((const char*)k + (j ? (u32)(64 * j - 4 + (rw & 3)) * (D_ * 2) + (u32)(64 * grp + 8 * c8) * 2 : toff));
    const v2u sk0 = *(const v2u*)(shk + (64 * grp + 16 * wq + (c & 7)) * 4), sk1 = *(const v2u*)(shk + (64 * grp + 16 * wq + 8 + (c & 7)) * 4);
    const v4u x0 = ldin((const char*)x + toff), x1 = ldin((const char*)x + toff + 32 * D_ * 2);
    u32 ep = __builtin_amdgcn_raw_buffer_load_b32(rs, OFF_FLG + FL_EPOCH * 128, 0, 0);
    const u16* hf = ssmk + (size_t)d * S_;
    const int tc = 64 * wh + l;
    const v4u hi0 = ldin(hf + 2040 - 8 * tc), hi1 = ldin(hf + 1016 - 8 * tc);
    const float dd = bf1(ssmD[d]);
    SB();
    if (t < 64) sy[t] = 0u;
    if (l < 32) *(LAS v4u*)(lds + L_U + wq * U_BYTES + l * 16) = v4u{0u, 0u, 0u, 0u};
    asm volatile("s_waitcnt lgkmcnt(0)" ::: "memory");
    __builtin_amdgcn_s_barrier();
    SB();
    const u16* hs = hf;
    asm volatile("" : "+s"(hs));
    v4u e0 = *(const CAS v4u*)(hs + 1528 - 512 * wh), e1 = *(const CAS v4u*)(hs + 504);
    SB();
    {
      const int cs = (c8 & 1) * 16;
      const int o0 = rw * 128 + (((c8 >> 1) ^ oct_sw(rw)) * 32) + cs;
      *(LAS v4u*)(lds + L_KIMG + (rw + 4) * 128 + (((c8 >> 1) ^ oct_sw(rw + 4)) * 32) + cs) = k0;
      *(LAS v4u*)(lds + L_KIMG + (rw + 36) * 128 + (((c8 >> 1) ^ oct_sw(rw + 4)) * 32) + cs) = k1;
      *(LAS v4u*)(lds + L_VIMG + o0) = v0;
      *(LAS v4u*)(lds + L_VIMG + o0 + 4096) = v1;
      if (j == 0) kh = v4u{0u, 0u, 0u, 0u};
      if (t < 32) *(LAS v4u*)(lds + L_KIMG + o0) = kh;
      *(LAS v4u*)(lds + L_OPX + o0) = x0;
      *(LAS v4u*)(lds + L_OPX + o0 + 4096) = x1;
      lds_signal(sy + SY_KV, l);
      lds_wait(sy + SY_KV, 4);
      const int r0 = 8 * g + q;
      const int pa = (p >> 1) * 4096 + (p & 1) * 8;
      const int ro = r0 * 128 + ((wq ^ oct_sw(r0)) * 32) + pa;
      LAS char* kp1 = lds + L_KIMG + (r0 + 8) * 128 + ((wq ^ oct_sw(r0 + 8)) * 32) + pa;
      const int so = (64 * grp + 16 * wq) * 4096 + j * 128, vo = (c & 7) * 4096 + (c >> 3) * 64 + g * 16;
#pragma unroll
      for (int u = 0; u < 2; ++u) {
        const v2u xa = tr_read(lds + L_OPX + ro + u * 16), xb = tr_read(lds + L_OPX + ro + u * 16 + 512);
        st16(rs, vo, OFF_XT + so + u * 32768, v4u{xa[0], xa[1], xb[0], xb[1]});
      }
#pragma unroll
      for (int u = 0; u < 2; ++u) {
        const v2u sk = u ? sk1 : sk0;
        const float a0 = bf_lo(sk[0]), a1 = bf_hi(sk[0]), a2 = bf_lo(sk[1]), a3 = bf_hi(sk[1]);
        const v2u ka = tr_read(lds + L_KIMG + ro + u * 16), kb = tr_read(lds + L_KIMG + ro + u * 16 + 512), kc = tr_read(kp1 + u * 16);
        const v2u va = tr_read(lds + L_VIMG + ro + u * 16), vb = tr_read(lds + L_VIMG + ro + u * 16 + 512);
        const float kf[12] = {0.f, bf_hi(ka[0]), bf_lo(ka[1]), bf_hi(ka[1]), bf_lo(kb[0]), bf_hi(kb[0]), bf_lo(kb[1]), bf_hi(kb[1]),
                              bf_lo(kc[0]), bf_hi(kc[0]), bf_lo(kc[1]), bf_hi(kc[1])};
        const u32 vw[4] = {va[0], va[1], vb[0], vb[1]};
        v4u o;
#pragma unroll
        for (int i = 0; i < 4; ++i) {
          float e0_ = kf[2 * i + 1] * a3 + kf[2 * i + 2] * a2 + kf[2 * i + 3] * a1 + kf[2 * i + 4] * a0;
          float e1_ = kf[2 * i + 2] * a3 + kf[2 * i + 3] * a2 + kf[2 * i + 4] * a1 + kf[2 * i + 5] * a0;
          u32 pk = pack2(e0_, e1_);
          o[i] = pack2(bf_lo(pk) * bf_lo(vw[i]), bf_hi(pk) * bf_hi(vw[i]));
        }
        st16(rs, vo, OFF_KVT + so + u * 32768, o);
      }
    }
    SB();
    const u32 tag = get_tag(ep);
    LAS char* fb = lds + L_FILT + ch * CH_FILT;
    {
      if (wh) e1 = v4u{0u, 0u, 0u, 0u};
      auto build = [&](v4u hi, v4u edge, int cidx) {
        u32 wd[8];
#pragma unroll
        for (int i = 0; i < 4; ++i) {
          wd[i] = __builtin_amdgcn_update_dpp((int)edge[i], (int)hi[i], 0x130, 0xf, 0xf, false);
          wd[4 + i] = hi[i];
        }
        u32 R[16];
#pragma unroll
        for (int n = 2; n < 16; ++n) {
          if (n & 1) R[n] = __builtin_amdgcn_alignbit(wd[n >> 1], wd[n >> 1], 16);
          else R[n] = (wd[n >> 1] & 0xffffu) | (wd[(n >> 1) - 1] & 0xffff0000u);
        }
#pragma unroll
        for (int qq = 0; qq < 8; ++qq) *(LAS v4u*)(fb + qq * CP_STRIDE + cidx * 16) = v4u{R[8 + qq], R[6 + qq], R[4 + qq], R[2 + qq]};
      };
      build(hi0, e0, tc);
      SB();
      publish<4>(sy + SY_A1CNT, flag_ptr(ws, FL_A + grp * 32 + j), tag, l);
      SB();
      build(hi1, e1, tc + 128);
      if (tc < 8) *(LAS v4u*)(fb + tc * CP_STRIDE + 256 * 16) = v4u{0u, 0u, 0u, 0u};
      lds_signal(sy + SY_FB0 + ch, l);
    }
    lds_wait(sy + SY_FB0 + ch, 2);
    v4u A[4][8];
    {
      LAS char* ap = fb + (c & 7) * CP_STRIDE + 2 * (136 - 8 * (c >> 3) + 8 * g - 128 * wh);
#pragma unroll
      for (int rr = 0; rr < 4; ++rr)
#pragma unroll
        for (int ip = 0; ip < 8; ++ip) A[rr][ip] = *(LAS v4u*)(ap + 2 * (32 * (3 - rr) + 256 * (7 - ip)));
      lds_signal(sy + SY_ARD, l);
      SB();
    }
    if (wq == 0) {
      poll_flags<2>(flag_ptr(ws, FL_A + grp * 32 + (l & 31)), l < 32, tag);
      lds_arrive(sy + SY_A1GO, l);
    } else {
      lds_wait(sy + SY_A1GO, 1);
    }
    LAS char* ub = lds + L_U + wq * U_BYTES;
    const int sl = 1024 * wh + 16 * c + 4 * g;
    v2u xq[4];
    v2u mq;
    const int mo_off = (q == 3 ? 4 : q) * 4096 + p * 24576;
    {
      v4u u[4];
#pragma unroll
      for (int i = 0; i < 4; ++i) { u[i] = ld16(rs, l * 16 + i * 1024, OFF_KVT + d * 4096); SB(); }
#pragma unroll
      for (int n = 0; n < 4; ++n) xq[n] = ld8(rs, sl * 2 + n * 512, OFF_XT + d * 4096);
      mq = ld8(rs, mo_off, OFF_MOD + d * 8);
      SB();
#pragma unroll
      for (int i = 0; i < 4; ++i) *(LAS v4u*)(ub + 512 + i * 1024 + l * 16) = u[i];
    }
    v4f acc[8];
#pragma unroll
    for (int n = 0; n < 8; ++n) acc[n] = v4f{0.f, 0.f, 0.f, 0.f};
    v2f hg[4][2], z0[4][2];
    v2f c1;
    {
      LAS char* bp = ub + 512 + 2 * (16 * c - 112 + 8 * g - 128 * wh);
      v4u B[2][8];
#pragma unroll
      for (int n = 0; n < 8; ++n) B[0][n] = *(LAS v4u*)(bp + 2 * (96 + 256 * n));
      SB();
      u32 rowf = 0;
      LAS char* kb = ub + 512 + 2 * sl;
      LAS char* rtb = lds + L_OPH + 4 * sl;
      asm volatile("" : "+v"(kb), "+v"(rtb));
#pragma unroll
      for (int rr = 0; rr < 4; ++rr) {
        if (rr == 2) {
          asm volatile("" ::: "memory");
          if (__any(mq[1] != tag)) mq = ld8(rs, mo_off, OFF_MOD + d * 8);
          if (__builtin_amdgcn_readfirstlane(rowf) < 2) lds_wait(sy + SY_ROW, 2);
          asm volatile("" ::: "memory");
          SB();
        }
        v2u kq[2];
        v4f ri[2], nm[2];
#pragma unroll
        for (int n = 0; n < 8; ++n) {
          if (n == 0 && rr < 3) {
#pragma unroll
            for (int m = 0; m < 8; ++m) B[(rr + 1) & 1][m] = *(LAS v4u*)(bp + 2 * (32 * (2 - rr) + 256 * m));
          }
          if (n == 3 && rr == 1) rowf = __hip_atomic_load(sy + SY_ROW, __ATOMIC_RELAXED, __HIP_MEMORY_SCOPE_WORKGROUP);
          if (n == 1 && rr >= 2) {
#pragma unroll
            for (int m = 0; m < 2; ++m) {
              kq[m] = *(LAS v2u*)(kb + 512 * (2 * (rr - 2) + m));
              ri[m] = *(LAS v4f*)(rtb + 1024 * (2 * (rr - 2) + m));
              nm[m] = *(LAS v4f*)(rtb + 8192 + 1024 * (2 * (rr - 2) + m));
            }
          }
          if (n >= 2 && n < 6 && rr >= 2) {
            const int m = (n - 2) >> 1, h = n & 1, t4 = 2 * (rr - 2) + m;
#pragma unroll
            for (int e = 0; e < 2; ++e) {
              float zz = (e ? bf_hi(kq[m][h]) : bf_lo(kq[m][h])) * dd;
              float hh = __builtin_fmaf(e ? bf_hi(xq[t4][h]) : bf_lo(xq[t4][h]), ri[m][2 * h + e], nm[m][2 * h + e]);
              asm volatile("" : "+v"(zz), "+v"(hh));
              z0[t4][h][e] = zz;
              hg[t4][h][e] = hh;
            }
          }
#pragma unroll
          for (int ip = 0; ip + n < 8; ++ip) acc[n + ip] = mfma16(A[rr][ip], B[rr & 1][n], acc[n + ip]);
#pragma unroll
          for (int ip = 0; ip + n < 8; ++ip) {
            __builtin_amdgcn_sched_group_barrier(0x8, 1, 0);
            if (n < 2) __builtin_amdgcn_sched_group_barrier(0x100, 1, 0);
            else if (n < 5) __builtin_amdgcn_sched_group_barrier(0x102, 2, 0);
            else __builtin_amdgcn_sched_group_barrier(0x102, 3, 0);
          }
          SB();
        }
      }
    }
    auto finish = [&](auto whc) {
      constexpr int WH = decltype(whc)::v;
#pragma unroll
      for (int n = 0; n < 4; ++n) *(LAS v4f*)(lds + L_XCH + wq * 4096 + (n * 64 + l) * 16) = acc[4 * (1 - WH) + n];
      lds_signal(sy + SY_PAIR0 + ch, l);
#pragma unroll
      for (int n = 0; n < 4; ++n) { z0[n][0] += lo2(acc[4 * WH + n]); z0[n][1] += hi2(acc[4 * WH + n]); }
    };
    if (wh) finish(IC<1>{}); else finish(IC<0>{});
    {
#pragma nounroll
      for (int spin = 0; spin < SPIN_CAP && __any(mq[1] != tag); ++spin) {
        asm volatile("" ::: "memory");
        mq = ld8(rs, mo_off, OFF_MOD + d * 8);
      }
      float mo = as_f(mq[0]);
      mo += dpp_mov<0xB1>(mo);
      mo = rbf(mo + dpp_mov<0x4E>(mo));
      const int mi = __builtin_bit_cast(int, mo);
      const float ga = as_f(__builtin_amdgcn_readlane(mi, 8));
      const v2f gc = dup2(ga * rbf(1.0f + as_f(__builtin_amdgcn_readlane(mi, 4)))), gs = dup2(ga * as_f(__builtin_amdgcn_readlane(mi, 0)));
      c1 = dup2(rbf(1.0f + as_f(__builtin_amdgcn_readlane(mi, 12))));
#pragma unroll
      for (int n = 0; n < 4; ++n)
#pragma unroll
        for (int e = 0; e < 2; ++e) hg[n][e] = fma2(hg[n][e], gc, gs);
    }
    lds_wait(sy + SY_PAIR0 + ch, 2);
    {
      const int vo = (c >> 2) * 65536 + ((c & 3) * 16 + 4 * g) * 2;
#pragma unroll
      for (int n = 0; n < 4; ++n) {
        const v4f y = *(LAS v4f*)(lds + L_XCH + (wq ^ 1) * 4096 + (n * 64 + l) * 16);
        const u32 p0 = pk2(fma2(hg[n][0], lo2(y) + z0[n][0], up2(xq[n][0]))), p1 = pk2(fma2(hg[n][1], hi2(y) + z0[n][1], up2(xq[n][1])));
        const int so = (16 * wh + 4 * n) * 65536 + d * 128;
        st8(rs, vo, OFF_UB + so, v2u{pk2(up2(p0) * c1), pk2(up2(p1) * c1)});
        st8(rs, vo, OFF_X1B + so, v2u{p0, p1});
      }
    }
    lds_signal(sy + SY_STO, l);
    publish_count<4>(sy + SY_BCNT, ws, FL_B, grp, l);
    const int voff = l * 16;
    v4u W[8][4];
    auto w2_load = [&](int n) { W[n >> 2][n & 3] = ld16(rs, l * 16 + (n & 3) * 1024, OFF_W2T + ((8 * wq + (n >> 2)) * 32 + 4 * cg) * 1024); };
    lds_wait(sy + SY_WOK, 1);
#pragma unroll
    for (int n = 0; n < W2B; ++n) w2_load(n);
    SB();
    lds_wait(sy + SY_BGO, 1);
    const int pb = OFF_PST + (rt * 4 + wq) * 2048;
    {
      const v4u xr0 = ld16(rs, voff, OFF_X1B + rt * 65536 + cg * 8192 + wq * 2048);
      const v4u xr1 = ld16(rs, voff + 1024, OFF_X1B + rt * 65536 + cg * 8192 + wq * 2048);
      v4u xs[SGC];
      stage_load<SGC>(xs, rs, rt, 64 - SGC * (4 - wq), l);
      SB();
      const int r8 = 16 * wq + (l >> 3);
      *(LAS v4u*)(lds + L_XS + r8 * 128 + ((((l & 7) >> 1) ^ oct_sw(r8)) * 32) + (l & 1) * 16) = xr0;
      *(LAS v4u*)(lds + L_XS + (r8 + 8) * 128 + ((((l & 7) >> 1) ^ oct_sw(r8 + 8)) * 32) + (l & 1) * 16) = xr1;
      lds_signal(sy + SY_XS, l);
      SB();
      stage_write<SGC>(xs, lds, 64 - SGC * (4 - wq), l);
      lds_wait(sy + SY_XS, 4);
      float s1 = 0.f, s2 = 0.f, s3 = 0.f;
      v2u xv[4];
#pragma unroll
      for (int i = 0; i < 4; ++i) {
        const int dl = 32 * (i >> 1) + 8 * g + 4 * (i & 1) + q;
        xv[i] = tr_read(lds + L_XS + dl * 128 + ((wq ^ oct_sw(dl)) * 32) + p * 8);
      }
#pragma unroll
      for (int i = 0; i < 4; ++i) {
        s1 = dot2(xv[i][0], 0x3f803f80u, s1);
        s1 = dot2(xv[i][1], 0x3f803f80u, s1);
      }
      s1 = xor32_add(xor16_add(s1));
      const float mt = s1 * (1.0f / 64);
#pragma unroll
      for (int i = 0; i < 4; ++i) { s2 = sqdev(xv[i][0], mt, s2); s3 = sqdev(xv[i][1], mt, s3); }
      s2 = xor32_add(xor16_add(s2 + s3));
      if (l < 16) st16(rs, l * 16, pb + cg * 256, v4u{as_u(s1), tag, as_u(s2), tag});
      WG_BARRIER();
    }
    if (b == 0 && t == 0) __hip_atomic_store(flag_ptr(ws, FL_EPOCH), tag, __ATOMIC_RELAXED, __HIP_MEMORY_SCOPE_AGENT);
    v4f acc1[3], tc1[3], tc2[3];
    v2f stt;
    {
#pragma unroll
      for (int a = 0; a < 3; ++a) {
        tc1[a] = *(LAS v4f*)(lds + L_TT + (8 + a) * 128 + 16 * g);
        tc2[a] = *(LAS v4f*)(lds + L_TT + (8 + a) * 128 + 64 + 16 * g);
      }
      SB();
#pragma unroll
      for (int a = 0; a < 3; ++a) acc1[a] = v4f{0.f, 0.f, 0.f, 0.f};
      LAS char* rb = lds + (4 * g + q) * 128 + ((wq ^ ((2 * g + (q >> 1)) & 3)) * 32) + p * 8;
      LAS char* ra = lds + L_W1C + l * 16;
      v4u B[2], A1[2][3], pa, pc;
      auto fetch = [&](int ks) {
        const v2u h0 = tr_read(rb + ks * 4096), h1 = tr_read(rb + ks * 4096 + 2048);
        B[ks & 1] = v4u{h0[0], h0[1], h1[0], h1[1]};
#pragma unroll
        for (int a = 0; a < 3; ++a) A1[ks & 1][a] = *(LAS v4u*)(ra + a * 16384 + ks * 1024);
      };
      fetch(0);
#pragma unroll
      for (int ks = 0; ks < 16; ++ks) {
        if (ks < 15) fetch(ks + 1);
        if (ks < PSTK) {
#pragma unroll
          for (int n = W2B + (32 - W2B) * ks / PSTK; n < W2B + (32 - W2B) * (ks + 1) / PSTK; ++n) w2_load(n);
        }
        if (ks == PSTK) { pa = ld16(rs, voff, pb); pc = ld16(rs, voff + 1024, pb); }
        SB();
#pragma unroll
        for (int a = 0; a < 3; ++a) acc1[a] = mfma16(A1[ks & 1][a], B[ks & 1], acc1[a]);
        SB();
        if (ks == PSTK + 5) {
          if (__any((tagdiff(pa, tag) | tagdiff(pc, tag)) != 0u)) wait_partials(rs, pb, l, tag, pa, pc);
          stt = combine_stats(pa, pc);
          asm volatile("" : "+v"(tc1[0]), "+v"(tc1[1]), "+v"(tc1[2]), "+v"(tc2[0]), "+v"(tc2[1]), "+v"(tc2[2]));
          if (l < 16) *(LAS v2f*)(lds + L_ST2 + (wq * 16 + l) * 8) = stt;
          lds_signal(sy + SY_ST2 + (wq >> 1), l);
          SB();
        }
      }
    }
    {
      const int base = OFF_HID + (4 * rt + wq) * NK2 * 1024;
      v2u o[3];
      const v2f r2 = dup2(stt[0]), m2 = dup2(stt[1]);
#pragma unroll
      for (int a = 0; a < 3; ++a) o[a] = act4(acc1[a], r2, m2, tc1[a], tc2[a]);
      st16(rs, voff, base + (32 + cg) * 1024, v4u{o[0][0], o[0][1], o[1][0], o[1][1]});
      st8(rs, cg < 6 ? l * 8 : OOB, base + 40 * 1024 + cg * 512, o[2]);
    }
    if (wq < 2) publish<6>(sy + SY_CCNT, flag_ptr(ws, FL_C + rt * 8 + cg), tag, l);
    else publish<6>(sy + SY_DCNT, flag_ptr(ws, FL_D + rt * 8 + cg), tag, l);
    __builtin_amdgcn_s_setprio(0);
    lds_wait(sy + SY_CCNT, 6);
    poll_flags<1>(flag_ptr(ws, FL_C + rt * 8 + 2 * wq + (l & 1)), l < 2, tag);
    lds_signal(sy + SY_C1GO + wq, l);
    {
      GAS u32* dfp = flag_ptr(ws, FL_D + rt * 8 + 2 * wq + (l & 1));
      u32 dfl = 0;
      v4u H[2][8];
      auto hload = [&](int s) {
#pragma unroll
        for (int i = 0; i < 8; ++i) H[s & 1][i] = ld16(rs, voff + (i & 3) * 1024, OFF_HID + ((4 * rt + s) * NK2 + 8 * wq + (i & 4)) * 1024);
      };
      hload(0);
      hload(1);
      SB();
#pragma unroll
      for (int s = 0; s < 4; ++s) {
        v4f acc2[4];
#pragma unroll
        for (int a = 0; a < 4; ++a) acc2[a] = v4f{0.f, 0.f, 0.f, 0.f};
#pragma unroll
        for (int i = 0; i < 8; ++i) {
#pragma unroll
          for (int a = 0; a < 4; ++a) acc2[a] = mfma16(W[i][a], H[s & 1][i], acc2[a]);
          SB();
        }
        if (s == 0) {
          lds_wait(sy + SY_DCNT, 6);
          dfl = __hip_atomic_load(dfp, __ATOMIC_RELAXED, __HIP_MEMORY_SCOPE_AGENT);
        }
        if (s == 1) {
          if (__any(dfl != tag)) poll_flags<1>(dfp, true, tag);
          lds_signal(sy + SY_CGO + wq, l);
          hload(2);
          hload(3);
        }
#pragma unroll
        for (int a = 0; a < 4; ++a) *(LAS v4f*)(lds + w * 16384 + (s * 4 + a) * 1024 + l * 16) = acc2[a];
        lds_signal(sy + SY_DMP + s, l);
        SB();
      }
    }
    lds_wait(sy + SY_DMP + 2 + (wq >> 1), 8);
    final_epilogue(lds, out, rt, cg, 2 + (wq >> 1), wq & 1, l);
  } else {
    const int l = lane_id(), tt = 64 * wq + l;
    const int g = l >> 4, q = (l & 15) >> 2, p = l & 3;
    u32 cpf = 0;
    {
      const int fo = l < 5 ? OFF_FLG + (b * 5 + l) * 128 : l < 13 ? OFF_PST + b * 1024 + (l & 7) * 128 : OFF_ST1 + (b >> 1) * 128;
      cpf = __builtin_amdgcn_raw_buffer_load_b32(rs, tt < 14 ? fo : OOB, 0, 0);
    }
    u32 ep = __builtin_amdgcn_raw_buffer_load_b32(rs, OFF_FLG + FL_EPOCH * 128, 0, 0);
    u32 bbase = __hip_atomic_load(flag_ptr(ws, FL_B + grp * 8 + (l & 7)), __ATOMIC_RELAXED, __HIP_MEMORY_SCOPE_AGENT);
    WG_BARRIER();
    LAS char* gsc = lds + L_GSC + wq * 4096;
    v4u tv[4], wv[4];
    auto tile_load = [&](v4u (&tv)[4], bool is_w1, int tk, int tc) {
      const rsrc_t rw = __builtin_amdgcn_make_buffer_rsrc((void*)(is_w1 ? w1 : w2), 0, F_ * D_ * 2, 0x00020000);
      const int ld = is_w1 ? F_ * 2 : D_ * 2, col = 64 * tc + 8 * (l & 7);
      const int ta = tk < 32 ? 2 * tk : tk < 38 ? 3 * tk - 32 : tk < 40 ? 2 * tk + 6 : 6 * tk - 174, tb = ta + (tk < 40 ? 1 : 3);
#pragma unroll
      for (int i = 0; i < 4; ++i)
        tv[i] = __builtin_amdgcn_raw_buffer_load_b128(rw, col < F_ ? (l >> 3) * ld + col * 2 : OOB, (16 * (i < 2 ? ta : tb) + 8 * (i & 1)) * ld, 0);
    };
    auto tile_store = [&](const v4u (&tv)[4], bool is_w1, int tk, int tc) {
#pragma unroll
      for (int i = 0; i < 4; ++i) *(LAS v4u*)(gsc + i * 1024 + l * 16) = tv[i];
      LAS char* rp = gsc + (4 * g + q) * 128 + (is_w1 ? 8 * p : 16 * p);
      const int dst = is_w1 ? OFF_W1T + (4 * tc * 16 + tk) * 1024 : OFF_W2T + (tk * 32 + 4 * tc) * 1024, dstep = is_w1 ? 16384 : 1024;
      v4u o[4];
#pragma unroll
      for (int a = 0; a < 4; ++a) {
        const int co = is_w1 ? 32 * a : 64 * (a >> 1) + 8 * (a & 1);
        const v2u h0 = tr_read(rp + co), h1 = tr_read(rp + co + 2048);
        o[a] = v4u{h0[0], h0[1], h1[0], h1[1]};
      }
#pragma unroll
      for (int a = 0; a < 4; ++a) st16(rs, 4 * tc + a < 86 ? l * 16 : OOB, dst + a * dstep, o[a]);
    };
    const int r0 = 8 * b + 2 * wq;
    const v4u xa = ldin(x + (size_t)r0 * D_ + l * 8);
    const v4u xb = ldin(x + (size_t)(r0 + 1) * D_ + l * 8);
    const bool do_mod = b >= 64;
    const int mcg = (b - 64) >> 2, mks = b & 3;
    const int task1 = 4 * b + wq;
    {
      const char* src = do_mod ? (const char*)wmod + (u32)(mks * 128 + (tt >> 3) * 4) * 6144u + (u32)(mcg * 128 + (tt & 7) * 16)
                               : (const char*)w1 + (u32)(32 * (task1 & 15) + (l >> 3)) * (F_ * 2) + (u32)((task1 >> 4) * 128 + (l & 7) * 16);
      const int ss = do_mod ? 6144 : 8 * F_ * 2;
#pragma unroll
      for (int i = 0; i < 4; ++i) wv[i] = ldin(src + i * ss);
    }
    const v2u c2 = *(const v2u*)(cvec + mks * 128 + (tt >> 3) * 4);
    SB();
    asm volatile("" : "+v"(bbase) : "v"(cpf));
    const u32 tag = get_tag(ep);
    {
      float s1 = 0.f, s2 = 0.f;
#pragma unroll
      for (int i = 0; i < 4; ++i) {
        s1 = dot2(xa[i], 0x3f803f80u, s1);
        s2 = dot2(xb[i], 0x3f803f80u, s2);
      }
      const float m1 = wave_sum(s1) * (1.0f / D_), m2 = wave_sum(s2) * (1.0f / D_);
      float q1 = 0.f, q2 = 0.f;
#pragma unroll
      for (int i = 0; i < 4; ++i) { q1 = sqdev(xa[i], m1, q1); q2 = sqdev(xb[i], m2, q2); }
      q1 = wave_sum(q1) * (1.0f / D_);
      q2 = wave_sum(q2) * (1.0f / D_);
      const u32 sA = ln_stats(l & 1 ? m2 : m1, l & 1 ? q2 : q1);
      st8(rs, l < 2 ? l * 8 : OOB, OFF_ST1 + r0 * 8, v2u{sA, tag});
    }
    const bool in_t = (wq == 1 || wq == 2) && b >= 32, do_t1 = in_t && b < 80, do_t2 = in_t && b >= 80 && 2 * j + wq - 21 < NK2;
    const int xt = 191 + 2 * b + wq;
    const int xk = do_t1 ? xt & 15 : 2 * j + wq - 21, xc = do_t1 ? xt >> 4 : grp;
    if (do_t1 || do_t2) tile_load(tv, do_t1, xk, xc);
    SB();
    if (do_mod) {
      v2f acc[4];
#pragma unroll
      for (int e = 0; e < 4; ++e) acc[e] = v2f{0.f, 0.f};
#pragma unroll
      for (int i = 0; i < 4; ++i) {
        const float c = i & 1 ? bf_hi(c2[i >> 1]) : bf_lo(c2[i >> 1]);
        const float s = rbf(c * __builtin_amdgcn_rcpf(1.0f + __expf(-c)));
#pragma unroll
        for (int e = 0; e < 4; ++e) acc[e] = fma2(up2(wv[i][e]), v2f{s, s}, acc[e]);
      }
      float r[8];
#pragma unroll
      for (int e = 0; e < 8; ++e) {
        const float a = acc[e >> 1][e & 1];
        r[e] = a + dpp_mov<0x128>(a);
      }
      if ((l & 8) == 0) {
        LAS char* mp = lds + L_MRED + (4 * wq + g) * 256 + (l & 7) * 32;
        *(LAS v4f*)mp = v4f{r[0], r[1], r[2], r[3]};
        *(LAS v4f*)(mp + 16) = v4f{r[4], r[5], r[6], r[7]};
      }
      lds_signal(sy + SY_MOD, l);
      if (wq == 0) {
        lds_wait(sy + SY_MOD, 4);
        LAS float* mr = (LAS float*)(lds + L_MRED) + l;
        float s = 0.f;
#pragma unroll
        for (int i = 0; i < 16; ++i) s += mr[64 * i];
        st8(rs, l * 8, OFF_MOD + (mks * 3072 + mcg * 64) * 8, v2u{as_u(s), tag});
      }
    } else {
      tile_store(wv, true, task1 & 15, task1 >> 4);
      publish<4>(sy + SY_XCNT, flag_ptr(ws, FL_T + b), tag, l);
    }
    if (do_t1 || do_t2) tile_store(tv, do_t1, xk, xc);
    if (in_t) publish<2>(sy + SY_TCNT, flag_ptr(ws, FL_T + 32 + b), tag, l);
    const int kp = 4 * cg + wq;
    const int sgw = sg0 + (wq < nsg ? wq : 0);
    const int voff = l * 16;
    v4u W1[16][2];
    auto w1_load = [&](int ks) {
      W1[ks][0] = ld16(rs, voff + (ks & 3) * 1024, OFF_W1T + 2 * kp * 16384 + (ks >> 2) * 4096);
      W1[ks][1] = ld16(rs, voff + (ks & 3) * 1024, OFF_W1T + (2 * kp + 1) * 16384 + (ks >> 2) * 4096);
    };
    const u32 b2v = b2[64 * cg + l];
    const u32 b1v = b1[l < 32 ? 32 * kp + l : 1024 + 16 * sgw + (l & 15)];
    v4u Cc[16];
    if (wq == 0 || wq == 3) {
      v4u sq[8];
      bool bad = true;
#pragma nounroll
      for (int spin = 0; spin < SPIN_CAP; ++spin) {
        asm volatile("" ::: "memory");
        if (bad) {
          u32 df = 0;
#pragma unroll
          for (int i = 0; i < 8; ++i) sq[i] = ld16(rs, voff + (i & 3) * 1024, OFF_ST1 + (wq & 1) * 8192 + (i >> 2) * 4096);
#pragma unroll
          for (int i = 0; i < 8; ++i) df |= tagdiff(sq[i], tag);
          bad = df != 0u;
        }
        if (!__any(bad)) break;
        __builtin_amdgcn_s_sleep(6);
      }
#pragma unroll
      for (int i = 0; i < 8; ++i) {
        const v2f ri = {__builtin_amdgcn_rcpf(bf_hi(sq[i][0])), __builtin_amdgcn_rcpf(bf_hi(sq[i][2]))};
        const v2f mu = {bf_lo(sq[i][0]), bf_lo(sq[i][2])};
        *(LAS v2f*)(lds + L_OPH + (wq & 1) * 4096 + i * 512 + l * 8) = ri;
        *(LAS v2f*)(lds + L_OPH + 8192 + (wq & 1) * 4096 + i * 512 + l * 8) = -mu * ri;
      }
      lds_signal(sy + SY_ROW, l);
    }
    poll_flags<6>(flag_ptr(ws, FL_T + 4 * (kp >> 1) + (l & 3)), l < 4, tag);
    lds_wait(sy + SY_A1GO, 1);
#pragma unroll
    for (int ks = 0; ks < 16; ++ks) w1_load(ks);
    SB();
    GAS u32* sfp = wq == 3 ? flag_ptr(ws, FL_U + 8 * (l < 22 ? l : 0) + cg) : flag_ptr(ws, FL_T + 64 + 8 * (sgw >> 2) + (l & 7));
    const u32 sfl = __hip_atomic_load(sfp, __ATOMIC_RELAXED, __HIP_MEMORY_SCOPE_AGENT);
    {
      v4u tq[8];
      v2u gq[4];
      bool bad = true;
#pragma nounroll
      for (int spin = 0; spin < SPIN_CAP; ++spin) {
        asm volatile("" ::: "memory");
        if (bad) {
          u32 df = 0;
#pragma unroll
          for (int i = 0; i < 8; ++i) tq[i] = ld16(rs, voff + (i & 1) * 1024, OFF_MOD + (i >> 1) * 24576 + 1536 * 8 + wq * 2048);
          if (wq == 3) {
#pragma unroll
            for (int i = 0; i < 4; ++i) gq[i] = ld8(rs, l * 8, OFF_MOD + i * 24576 + (2560 + 64 * cg) * 8);
#pragma unroll
            for (int i = 0; i < 4; ++i) df |= gq[i][1] ^ tag;
          }
#pragma unroll
          for (int i = 0; i < 8; ++i) df |= tagdiff(tq[i], tag);
          bad = df != 0u;
        }
        if (!__any(bad)) break;
        __builtin_amdgcn_s_sleep(4);
      }
      if (wq == 3) {
        float g01 = as_f(gq[0][0]) + as_f(gq[1][0]);
        asm volatile("" : "+v"(g01));
        float gs = g01 + (as_f(gq[2][0]) + as_f(gq[3][0]));
        asm volatile("" : "+v"(gs));
        *(LAS float*)(lds + L_GB + l * 4) = rbf(gs);
        *(LAS float*)(lds + L_GB + 256 + l * 4) = bf_lo(b2v);
      }
#pragma unroll
      for (int h = 0; h < 2; ++h) {
        const v2f sm = {(as_f(tq[h][0]) + as_f(tq[2 + h][0])) + (as_f(tq[4 + h][0]) + as_f(tq[6 + h][0])),
                        (as_f(tq[h][2]) + as_f(tq[2 + h][2])) + (as_f(tq[4 + h][2]) + as_f(tq[6 + h][2]))};
        u32 pk = pk2(sm);
        if (wq >= 2) pk = pk2(up2(pk) + 1.0f);
        *(LAS u32*)(lds + L_TB + wq * 512 + h * 256 + (l >> 4) * 64 + ((l >> 1) & 3) * 16 + ((l >> 3) & 1) * 8 + (l & 1) * 4) = pk;
      }
      lds_signal(sy + SY_TAB, l);
      if (__any(sfl != tag)) poll_flags<4>(sfp, true, tag);
      if (wq == 3) lds_arrive(sy + SY_WOK, l);
      lds_wait(sy + SY_STO, 4);
      if (wq < nsg) {
#pragma unroll
        for (int ks = 0; ks < 16; ++ks) Cc[ks] = ld16(rs, voff + (ks & 3) * 1024, OFF_W1T + (64 + sgw) * 16384 + (ks >> 2) * 4096);
      } else {
#pragma unroll
        for (int ks = 0; ks < 16; ++ks) Cc[ks] = v4u{0u, 0u, 0u, 0u};
      }
      SB();
      lds_wait(sy + SY_TAB, 4);
      lds_wait(sy + SY_ARD, 4);
      v4f tacc[3];
#pragma unroll
      for (int a = 0; a < 3; ++a) tacc[a] = v4f{0.f, 0.f, 0.f, 0.f};
      LAS char* tbp = lds + L_TB + (((l >> 3) & 1) ^ 1) * 1024 + 16 * g;
#pragma unroll
      for (int h = 0; h < 2; ++h) {
        v4u tb[8];
#pragma unroll
        for (int i = 0; i < 8; ++i) tb[i] = tb_frag(tbp, 8 * h + i);
        SB();
#pragma unroll
        for (int i = 0; i < 8; ++i) {
          tacc[0] = mfma16(W1[8 * h + i][0], tb[i], tacc[0]);
          tacc[1] = mfma16(W1[8 * h + i][1], tb[i], tacc[1]);
        }
        SB();
      }
      if (wq < 3) {
#pragma unroll
        for (int h = 0; h < 2; ++h) {
          v4u tb[8];
#pragma unroll
          for (int i = 0; i < 8; ++i) tb[i] = tb_frag(tbp, 8 * h + i);
          SB();
#pragma unroll
          for (int i = 0; i < 8; ++i) {
            tacc[2] = mfma16(Cc[8 * h + i], tb[i], tacc[2]);
            *(LAS v4u*)(lds + L_W1C + wq * 16384 + (8 * h + i) * 1024 + voff) = Cc[8 * h + i];
          }
          SB();
        }
      }
      if ((l & 7) == 0) {
#pragma unroll
        for (int a = 0; a < 3; ++a) *(LAS v4f*)(lds + L_TT + (a < 2 ? 2 * wq + a : 8 + wq) * 128 + (l & 8) * 8 + 16 * g) = tacc[a];
      }
      if (l < 48) *(LAS float*)(lds + L_TT + (l < 32 ? 2 * wq + (l >> 4) : 8 + wq) * 128 + 64 + (l & 15) * 4) += bf_lo(b1v);
    }
    if (wq == 3) {
      lds_wait(sy + SY_BCNT, 4);
      poll_flags<2>(flag_ptr(ws, FL_B + grp * 8 + (l & 7)), l < 8, bbase + 32u);
      lds_arrive(sy + SY_BGO, l);
    } else {
      lds_wait(sy + SY_BGO, 1);
    }
    v4u W[3][4];
    {
      v4u xs[16 - SGC];
      stage_load<16 - SGC>(xs, rs, rt, (16 - SGC) * wq, l);
      SB();
      lds_wait(sy + SY_WOK, 1);
#pragma unroll
      for (int n = 0; n < 3; ++n) {
        const int kap = n < 2 ? 32 + 2 * wq + n : 40 + wq;
#pragma unroll
        for (int i = 0; i < 4; ++i) W[n][i] = ld16(rs, (kap < NK2 ? voff : OOB) + i * 1024, OFF_W2T + (kap * 32 + 4 * cg) * 1024);
      }
      SB();
      stage_write<16 - SGC>(xs, lds, (16 - SGC) * wq, l);
    }
    WG_BARRIER();
    const int c = l & 15;
    const int sw = (2 * g + (q >> 1)) & 3;
    auto phase = [&](auto hc) {
      constexpr int h = decltype(hc)::v, S0 = 2 * h, NS = 2, DP = 3;
      v4f acc1[2][NS];
      LAS char* rb[NS];
#pragma unroll
      for (int i = 0; i < NS; ++i) {
        rb[i] = lds + (4 * g + q) * 128 + (((S0 + i) ^ sw) * 32) + p * 8;
        acc1[0][i] = acc1[1][i] = v4f{0.f, 0.f, 0.f, 0.f};
      }
      v4u B[DP][NS];
      auto fetch = [&](int ks) {
#pragma unroll
        for (int i = 0; i < NS; ++i) {
          v2u h0 = tr_read(rb[i] + ks * 4096), h1 = tr_read(rb[i] + ks * 4096 + 2048);
          B[ks % DP][i] = v4u{h0[0], h0[1], h1[0], h1[1]};
        }
      };
#pragma unroll
      for (int ks = 0; ks < DP - 1; ++ks) fetch(ks);
#pragma unroll
      for (int ks = 0; ks < 16; ++ks) {
        SB();
        if (h == 1 && ks == PUBK) {
          publish<6>(sy + SY_CCNT, flag_ptr(ws, FL_C + rt * 8 + cg), tag, l);
          SB();
        }
        if (ks + DP - 1 < 16) fetch(ks + DP - 1);
#pragma unroll
        for (int i = 0; i < NS; ++i) {
          acc1[0][i] = mfma16(W1[ks][0], B[ks % DP][i], acc1[0][i]);
          acc1[1][i] = mfma16(W1[ks][1], B[ks % DP][i], acc1[1][i]);
        }
#pragma unroll
        for (int i = 0; i < 2 * NS; ++i) {
          __builtin_amdgcn_sched_group_barrier(0x8, 1, 0);
          __builtin_amdgcn_sched_group_barrier(0x100, 1, 0);
        }
      }
      SB();
      v4f tq[2][2];
#pragma unroll
      for (int a = 0; a < 2; ++a) {
        tq[a][0] = *(LAS v4f*)(lds + L_TT + (2 * wq + a) * 128 + 16 * g);
        tq[a][1] = *(LAS v4f*)(lds + L_TT + (2 * wq + a) * 128 + 64 + 16 * g);
      }
      lds_wait(sy + SY_ST2 + h, NS);
#pragma unroll
      for (int i = 0; i < NS; ++i) {
        const v2f stt = *(LAS v2f*)(lds + L_ST2 + ((S0 + i) * 16 + c) * 8);
        const v2f r2 = dup2(stt[0]), m2 = dup2(stt[1]);
        const v2u o0 = act4(acc1[0][i], r2, m2, tq[0][0], tq[0][1]), o1 = act4(acc1[1][i], r2, m2, tq[1][0], tq[1][1]);
        st16(rs, voff, OFF_HID + ((4 * rt + S0 + i) * NK2 + kp) * 1024, v4u{o0[0], o0[1], o1[0], o1[1]});
      }
      SB();
    };
    phase(IC<0>{});
    __builtin_amdgcn_s_setprio(3);
    phase(IC<1>{});
    publish<6>(sy + SY_DCNT, flag_ptr(ws, FL_D + rt * 8 + cg), tag, l);
    {
      v4u H[4][3];
      auto hload = [&](int s) {
        const int so = OFF_HID + (4 * rt + s) * NK2 * 1024;
        H[s][0] = ld16(rs, voff, so + (32 + 2 * wq) * 1024);
        H[s][1] = ld16(rs, voff + 1024, so + (32 + 2 * wq) * 1024);
        const int vo = wq < 3 ? l * 8 : OOB;
        const v2u h0 = ld8(rs, vo, so + (40 + wq) * 1024), h1 = ld8(rs, vo + 512, so + (40 + wq) * 1024);
        H[s][2] = v4u{h0[0], h0[1], h1[0], h1[1]};
      };
      auto part = [&](int s) {
        v4f acc2[4];
#pragma unroll
        for (int a = 0; a < 4; ++a) acc2[a] = v4f{0.f, 0.f, 0.f, 0.f};
#pragma unroll
        for (int i = 0; i < 3; ++i)
#pragma unroll
          for (int a = 0; a < 4; ++a) acc2[a] = mfma16(W[i][a], H[s][i], acc2[a]);
#pragma unroll
        for (int a = 0; a < 4; ++a) *(LAS v4f*)(lds + w * 16384 + (s * 4 + a) * 1024 + l * 16) = acc2[a];
        lds_signal(sy + SY_DMP + s, l);
        SB();
      };
      lds_wait(sy + SY_C1GO + wq, 1);
      hload(0);
      hload(1);
      SB();
      lds_wait(sy + SY_DCNT, 6);
      part(0);
      part(1);
      lds_wait(sy + SY_CGO + wq, 1);
      hload(2);
      hload(3);
      SB();
      part(2);
      part(3);
    }
    lds_wait(sy + SY_DMP + (wq >> 1), 8);
    final_epilogue(lds, out, rt, cg, wq >> 1, wq & 1, l);
  }
}

extern "C" void kernel_launch(void* const* d_in, const int* in_sizes, int n_in, void* d_out, int out_size,
                              void* d_ws, size_t ws_size, hipStream_t stream) {
  fused_block<<<256, 512, 0, stream>>>((const u16*)d_in[0], (const u16*)d_in[1], (const u16*)d_in[2], (const u16*)d_in[3],
                                       (const u16*)d_in[4], (const u16*)d_in[5], (const u16*)d_in[7], (const u16*)d_in[8],
                                       (const u16*)d_in[9], (const u16*)d_in[10], (const u16*)d_in[11], (const u16*)d_in[12],
                                       (char*)d_ws, (u16*)d_out);
}
```
